```python
import torch

def _softmax(x, axis=-1):
    return torch.softmax(x, dim=axis)
def _gelu(x):
    return torch.nn.functional.gelu(x, approximate="tanh")
def _silu(x):
    return torch.nn.functional.silu(x)
def _sigmoid(x):
    return torch.sigmoid(x)
def _layernorm(x, axis=-1, eps=1e-5):
    m = x.mean(dim=axis, keepdim=True)
    v = x.var(dim=axis, keepdim=True, unbiased=False)
    return (x - m) / torch.sqrt(v + eps)
def _scatter_add(tgt, idx, src, axis=0):
    return tgt.clone().index_add_(axis, idx, src)
def _segment_sum(src, seg, n):
    out = torch.zeros((n,) + src.shape[1:], dtype=src.dtype)
    return out.index_add_(0, seg, src)
def _pad(x, pw):
    flat = tuple(v for pair in reversed(pw) for v in pair)
    return torch.nn.functional.pad(x, flat)

kernel_name = 'gated_mix__ssm_conv_h3__dense_activation_mish__prompt'

def setup_inputs():

    _w = (lambda a: (lambda t: t.to(torch.bfloat16) if t.is_floating_point() else t)(torch.as_tensor(a)))
    def _A_setup():
        B, S, D = 1, 2048, 448
        ks = 4
        return {
            "q": _w((lambda *s: torch.randn(*s, dtype=torch.bfloat16) * 0.05)(B, S, D)),
            "k": _w((lambda *s: torch.randn(*s, dtype=torch.bfloat16) * 0.4)(B, S, D)),
            "v": _w((lambda *s: torch.randn(*s, dtype=torch.bfloat16) * 0.2)(B, S, D)),
            "shift_k": _w((lambda *s: torch.randn(*s, dtype=torch.bfloat16) * 0.4)(D, ks) * 0.5),
            "ssm_k": _w((lambda *s: torch.randn(*s, dtype=torch.bfloat16) * 0.4)(D, S) * (1.0 / S ** 0.5)),
            "ssm_D": _w((lambda *s: torch.randn(*s, dtype=torch.bfloat16) * 0.2)(D)),
        }
    _A_in = _A_setup()

    def _B_setup():
        B, S, D, F = 1, 2048, 448, 1792
        return {
            "x": _w((lambda *s: torch.randn(*s, dtype=torch.bfloat16) * 0.05)(B, S, D)),
            "w1": _w((lambda *s: torch.randn(*s, dtype=torch.bfloat16) * 3.2)(D, F) * (D ** -0.5)),
            "b1": _w((lambda *s: torch.randn(*s, dtype=torch.bfloat16) * 0.05)(F)),
            "w2": _w((lambda *s: torch.randn(*s, dtype=torch.bfloat16) * 0.05)(F, D) * (F ** -0.5)),
        }
    _B_in = _B_setup()

    return {
        "A_q": _A_in["q"],
        "A_k": _A_in["k"],
        "A_v": _A_in["v"],
        "A_shift_k": _A_in["shift_k"],
        "A_ssm_k": _A_in["ssm_k"],
        "A_ssm_D": _A_in["ssm_D"],
        "B_x": _B_in["x"],
        "B_w1": _B_in["w1"],
        "B_b1": _B_in["b1"],
        "B_w2": _B_in["w2"],
        "gate_w": _w((lambda *s: torch.randn(*s, dtype=torch.bfloat16) * 0.05)(448) * 16.0),
    }

def reference(A_q, A_k, A_v, A_shift_k, A_ssm_k, A_ssm_D, B_x, B_w1, B_b1, B_w2, gate_w):
    def _A_ref(q, k, v, shift_k, ssm_k, ssm_D):
        B, S, D = q.shape
        ks = shift_k.shape[-1]
        k_pad = _pad(k, ((0, 0), (ks - 1, 0), (0, 0)))
        k_shift = torch.zeros_like(k)
        for t in range(ks):
            k_shift = k_shift + k_pad[:, t:t + S, :] * shift_k[:, ks - 1 - t][None, None, :]
        kv = k_shift * v
        L = 2 * S
        kv_pad = _pad(kv, ((0, 0), (0, S), (0, 0)))
        kf_pad = _pad(ssm_k, ((0, 0), (0, S)))
        kv_f = (lambda x, axes=(-2,-1): torch.fft.rfft2(x.float(), dim=axes))(kv_pad[:, None, :, :], axes=(1, 2))
        kf_f = (lambda x, axes=(-2,-1): torch.fft.rfft2(x.float(), dim=axes))(kf_pad[:, None, :], axes=(1, 2))
        kf_f = (lambda x, *d: x.permute(*d))(kf_f, 1, 2, 0)
        y_f = kv_f * kf_f[None, :, :, :]
        y = (lambda x, s, axes=(-2,-1): torch.fft.irfft2(x, s=s, dim=axes).to(torch.bfloat16))(y_f, (1, L), axes=(1, 2))[:, 0, :S, :]
        y = y + kv * ssm_D[None, None, :]
        return q * y

    def _B_ref(x, w1, b1, w2):
        h = torch.einsum("bsd,df->bsf", x, w1) + b1
        sp = torch.log1p(torch.exp(-torch.abs(h))) + torch.where(h > 0, h, torch.zeros_like(h))
        act = h * torch.tanh(sp)
        return torch.einsum("bsf,fd->bsd", act, w2)

    x = A_q
    g = _sigmoid((x * gate_w).sum(-1, keepdims=True))
    a = _A_ref(x, A_k, A_v, A_shift_k, A_ssm_k, A_ssm_D)
    b = _B_ref(x, B_w1, B_b1, B_w2)
    return g * a + (1 - g) * b
```

```cpp
#include <hip/hip_runtime.h>

typedef unsigned int u32;
typedef unsigned long long u64;
typedef __attribute__((ext_vector_type(2))) __bf16 b2;
typedef __attribute__((ext_vector_type(4))) __bf16 b4;
typedef __attribute__((ext_vector_type(8))) __bf16 b8;
typedef __attribute__((ext_vector_type(2))) unsigned u2;
typedef __attribute__((ext_vector_type(4))) unsigned u4;
typedef __attribute__((ext_vector_type(2))) float f2v;
typedef __attribute__((ext_vector_type(4))) float f4v;
typedef __attribute__((ext_vector_type(16))) float f16v;
typedef __amdgpu_buffer_rsrc_t rsrc_t;
#define LDSP(T, a) ((__attribute__((address_space(3))) T*)(size_t)(a))
#define DI __device__ __forceinline__

#define OFF_KVF 1024u
#define OFF_G 16384u
#define OFF_HF 32768u
#define OFF_YF 49152u
#define OFF_KVR 65536u
#define OFF_Y2 (OFF_KVR + 448u * 4096u + 4096u)
#define OFF_H (OFF_Y2 + 448u * 4096u + 4096u)
#define WS_USED (OFF_H + 2048u * 1792u * 2u)
#define SPIN_LIM 400000

#define STAMP(i) do { } while (0)

DI float bflo(u32 v) { return __uint_as_float(v << 16); }
DI float bfhi(u32 v) { return __uint_as_float(v & 0xffff0000u); }
DI u32 pack2(float lo, float hi) {
  f2v f = {lo, hi};
  b2 r = __builtin_convertvector(f, b2);
  return __builtin_bit_cast(u32, r);
}
DI rsrc_t mkrs(const void* p, u32 bytes) { return __builtin_amdgcn_make_buffer_rsrc((void*)p, 0, bytes, 0x00020000); }
DI void glds16(u32 ldsaddr, u32 voff, rsrc_t rs, u32 soff) {
  asm volatile("s_mov_b32 m0, %0\n\ts_nop 0\n\tbuffer_load_dwordx4 %1, %2, %3 offen lds" ::"s"(ldsaddr), "v"(voff), "s"(rs), "s"(soff) : "memory");
}
DI u4 aload16(u32 voff, rsrc_t rs, u32 soff) {
  u4 r;
  asm volatile("buffer_load_dwordx4 %0, %1, %2, %3 offen" : "=v"(r) : "v"(voff), "s"(rs), "s"(soff) : "memory");
  return r;
}
DI u2 aload8(u32 voff, rsrc_t rs, u32 soff) {
  u2 r;
  asm volatile("buffer_load_dwordx2 %0, %1, %2, %3 offen" : "=v"(r) : "v"(voff), "s"(rs), "s"(soff) : "memory");
  return r;
}
DI void bar() { asm volatile("s_waitcnt lgkmcnt(0)\n\ts_barrier" ::: "memory"); }
DI void vm0() { asm volatile("s_waitcnt vmcnt(0)" ::: "memory"); }
DI void flag_set(char* ws, u32 off, u64 tag) {
  __hip_atomic_store((u64*)(ws + off), tag, __ATOMIC_RELAXED, __HIP_MEMORY_SCOPE_AGENT);
}
DI u64 flag_get(char* ws, u32 off) { return __hip_atomic_load((u64*)(ws + off), __ATOMIC_RELAXED, __HIP_MEMORY_SCOPE_AGENT); }
DI void poll(char* ws, u32 off, u64 tag) {
  for (int it = 0; it < SPIN_LIM; ++it) {
    u64 v = flag_get(ws, off);
    if (__all(v == tag)) break;
    __builtin_amdgcn_s_sleep(1);
  }
  asm volatile("" ::: "memory");
}
DI b8 cat8(b4 a, b4 c) { return __builtin_shufflevector(a, c, 0, 1, 2, 3, 4, 5, 6, 7); }
DI b4 trread(u32 a) { return __builtin_amdgcn_ds_read_tr16_b64_v4bf16(LDSP(b4, a)); }

DI float mish(float h) {
  float e = __builtin_amdgcn_exp2f(h * 1.44269504088896f);
  const float c = 0.70710678118654752f;
  float u = __builtin_fmaf(e, c, c);
  float r = __builtin_amdgcn_rcpf(__builtin_fmaf(u, u, 0.5f));
  return __builtin_fmaf(-h, r, h);
}

template <int Q>
DI void red_write(const f4v (&a)[8], u32 rb, int lane) {
#pragma unroll
  for (int o = 0; o < 4; ++o) {
    if (o == Q) continue;
    const int op = (o - Q - 1) & 3;
    *LDSP(f4v, rb + (Q * 6 + op * 2) * 1024 + lane * 16) = a[2 * o];
    *LDSP(f4v, rb + (Q * 6 + op * 2 + 1) * 1024 + lane * 16) = a[2 * o + 1];
  }
}
template <int Q>
DI void red_read(const f4v (&a)[8], u32 rb, int lane, f4v& r0, f4v& r1) {
  r0 = a[2 * Q];
  r1 = a[2 * Q + 1];
#pragma unroll
  for (int s = 0; s < 4; ++s) {
    if (s == Q) continue;
    const int op = (Q - s - 1) & 3;
    r0 += *LDSP(f4v, rb + (s * 6 + op * 2) * 1024 + lane * 16);
    r1 += *LDSP(f4v, rb + (s * 6 + op * 2 + 1) * 1024 + lane * 16);
  }
}
DI void red_w(int q, const f4v (&a)[8], u32 rb, int lane) {
  if (q == 0) red_write<0>(a, rb, lane);
  else if (q == 1) red_write<1>(a, rb, lane);
  else if (q == 2) red_write<2>(a, rb, lane);
  else red_write<3>(a, rb, lane);
}
DI void red_r(int q, const f4v (&a)[8], u32 rb, int lane, f4v& r0, f4v& r1) {
  if (q == 0) red_read<0>(a, rb, lane, r0, r1);
  else if (q == 1) red_read<1>(a, rb, lane, r0, r1);
  else if (q == 2) red_read<2>(a, rb, lane, r0, r1);
  else red_read<3>(a, rb, lane, r0, r1);
}

#define CV_K 114688u
#define CV_R 131584u
#define KST 4224u
#define RST 4576u

__global__ __launch_bounds__(512, 2) void fused_kernel(const char* __restrict__ Q, const char* __restrict__ Kin, const char* __restrict__ V,
                                                       const char* __restrict__ SK, const char* __restrict__ SSMK,
                                                       const char* __restrict__ SSMD, const char* __restrict__ W1,
                                                       const char* __restrict__ B1, const char* __restrict__ W2,
                                                       const char* __restrict__ GW, char* __restrict__ out, char* __restrict__ ws) {
  __shared__ __attribute__((aligned(16))) char smem[163840];
  const u32 sm = (u32)(size_t)smem;
  const int tid = threadIdx.x, lane = tid & 63;
  const int w = __builtin_amdgcn_readfirstlane(tid >> 6);
  const rsrc_t rsQ = mkrs(Q, 2048u * 896u);
  const rsrc_t rsW = mkrs(ws, WS_USED);

  if (blockIdx.x < 32) {
    const u64 tag = flag_get(ws, 0) + 1;
    const int row0 = blockIdx.x * 64 + w * 8;
    const u32 lo = lane < 56 ? lane * 16u : 0x80000000u;
    u4 gw = __builtin_amdgcn_raw_buffer_load_b128(mkrs(GW, 896u), lo, 0, 0);
    u4 xr[8];
#pragma unroll
    for (int i = 0; i < 8; ++i) xr[i] = __builtin_amdgcn_raw_buffer_load_b128(rsQ, lo, (row0 + i) * 896u, 0);
    float mine = 0.f;
#pragma unroll
    for (int i = 0; i < 8; ++i) {
      float s = 0.f;
#pragma unroll
      for (int e = 0; e < 4; ++e) s += bflo(xr[i][e]) * bflo(gw[e]) + bfhi(xr[i][e]) * bfhi(gw[e]);
#pragma unroll
      for (int m = 1; m < 64; m <<= 1) s += __shfl_xor(s, m);
      if (lane == i) mine = s;
    }
    if (lane < 8) {
      float g = __builtin_amdgcn_rcpf(1.f + __builtin_amdgcn_exp2f(-1.44269504088896f * mine));
      u64 gr = (tag << 16) | (u64)(pack2(g, 0.f) & 0xffffu);
      flag_set(ws, OFF_G + (row0 + lane) * 8u, gr);
    }
    if (blockIdx.x == 0) {
      for (int i = 0; i < 4; ++i) {
        int f = tid + i * 512;
        poll(ws, OFF_HF + (f < 1792 ? f : 0) * 8u, tag);
      }
      for (int it = 0; it < SPIN_LIM; ++it) {
        u64 v = flag_get(ws, OFF_G + (tid & 255) * 64u);
        if (__all((v >> 16) == (tag & 0xffffffffffffull))) break;
        __builtin_amdgcn_s_sleep(2);
      }
      __syncthreads();
      if (tid == 0) flag_set(ws, 0, tag);
    }
    return;
  }

  const int b = blockIdx.x - 32;
  const int xcd = b & 7, idx = b >> 3;
  const int mt = 2 * xcd + idx / 14, ft = idx % 14;
  const int tb = 4 * xcd + idx / 7, cb = idx % 7;
  const int h = lane >> 5, r31 = lane & 31;
  const int blk = (lane >> 4) & 1, q4 = (lane & 15) >> 2, p4 = lane & 3;
  STAMP(0);

  const int fg = w & 3, mh = w >> 2;
  u4 stg[16];
  u2 bias[4];
  u4 kreg[2], rreg[2];
  u32 rext[2];
  u64 tag;
  const rsrc_t rsW1 = mkrs(W1, 448u * 3584u);
  {
    const rsrc_t rsB1 = mkrs(B1, 3584u);
#pragma unroll
    for (int i = 0; i < 4; ++i) bias[i] = aload8((ft * 128 + fg * 32 + 8 * i + 4 * h) * 2u, rsB1, 0);
  }
  if (w < 4) {
    u32 xo[4], wo[4];
#pragma unroll
    for (int i = 0; i < 4; ++i) {
      int row = 32 * w + 8 * i + (lane >> 3);
      xo[i] = (mt * 128 + row) * 896u + (((lane & 7) ^ ((row >> 1) & 7)) * 16u);
      int rw = 16 * w + 4 * i + (lane >> 4);
      wo[i] = rw * 3584u + ft * 256u + (((lane & 15) ^ (((rw & 3) << 2) | ((rw >> 2) & 3))) * 16u);
    }
#pragma unroll
    for (int kb = 0; kb < 5; ++kb) {
#pragma unroll
      for (int i = 0; i < 4; ++i) {
        glds16(sm + kb * 32768u + (4 * w + i) * 1024u, xo[i], rsQ, kb * 128u);
        glds16(sm + kb * 32768u + 16384u + (4 * w + i) * 1024u, wo[i], rsW1, kb * 64u * 3584u);
      }
    }
#pragma unroll
    for (int kb = 5; kb < 7; ++kb) {
#pragma unroll
      for (int i = 0; i < 4; ++i) {
        stg[(kb - 5) * 8 + 2 * i] = aload16(xo[i], rsQ, kb * 128u);
        stg[(kb - 5) * 8 + 2 * i + 1] = aload16(wo[i], rsW1, kb * 64u * 3584u);
      }
    }
  } else {
    tag = flag_get(ws, 0) + 1;
    const int pw = w - 4;
    const int oct = lane & 7, cp = 8 * pw + (lane >> 3);
    const int c = 64 * cb + 2 * cp, t0 = 64 * tb + 8 * oct;
    const rsrc_t rsK = mkrs(Kin, 2048u * 896u), rsV = mkrs(V, 2048u * 896u);
    u32 kk[11], vv[8];
    const u32 o0 = t0 * 896u + c * 2u;
#pragma unroll
    for (int i = 0; i < 3; ++i) kk[i] = __builtin_amdgcn_raw_buffer_load_b32(rsK, t0 > 0 ? o0 - (3 - i) * 896u : 0x80000000u, 0, 0);
#pragma unroll
    for (int i = 0; i < 8; ++i) kk[3 + i] = __builtin_amdgcn_raw_buffer_load_b32(rsK, o0 + i * 896u, 0, 0);
#pragma unroll
    for (int i = 0; i < 8; ++i) vv[i] = __builtin_amdgcn_raw_buffer_load_b32(rsV, o0 + i * 896u, 0, 0);
    u4 sk = __builtin_amdgcn_raw_buffer_load_b128(mkrs(SK, 3584u), c * 8u, 0, 0);
    const rsrc_t rsSK = mkrs(SSMK, 448u * 4096u);
#pragma unroll
    for (int i = 0; i < 2; ++i) kreg[i] = __builtin_amdgcn_raw_buffer_load_b128(rsSK, (tid - 256) * 16u, (2 * b + i) * 4096u, 0);
    float a0[8], a1[8];
#pragma unroll
    for (int r = 0; r < 8; ++r) {
      float s0 = bflo(kk[r]) * bfhi(sk[1]) + bflo(kk[r + 1]) * bflo(sk[1]) + bflo(kk[r + 2]) * bfhi(sk[0]) + bflo(kk[r + 3]) * bflo(sk[0]);
      float s1 = bfhi(kk[r]) * bfhi(sk[3]) + bfhi(kk[r + 1]) * bflo(sk[3]) + bfhi(kk[r + 2]) * bfhi(sk[2]) + bfhi(kk[r + 3]) * bflo(sk[2]);
      a0[r] = s0 * bflo(vv[r]);
      a1[r] = s1 * bfhi(vv[r]);
    }
    u4 o0v = {pack2(a0[7], a0[6]), pack2(a0[5], a0[4]), pack2(a0[3], a0[2]), pack2(a0[1], a0[0])};
    u4 o1v = {pack2(a1[7], a1[6]), pack2(a1[5], a1[4]), pack2(a1[3], a1[2]), pack2(a1[1], a1[0])};
    const u32 so = OFF_KVR + c * 4096u + (2040 - t0) * 2u;
    __builtin_amdgcn_raw_buffer_store_b128(o0v, rsW, so, 0, 16);
    __builtin_amdgcn_raw_buffer_store_b128(o1v, rsW, so + 4096u, 0, 16);
    vm0();
    if (lane == 0) flag_set(ws, OFF_KVF + ((cb * 4 + pw) * 32 + tb) * 8u, tag);
  }
  STAMP(1);

  f16v acc0, acc1;
  const u32 xa0 = (64 * mh + r31) * 128u, xk = (u32)((r31 >> 1) & 7);
  u32 wa[2];
#pragma unroll
  for (int t = 0; t < 2; ++t)
    wa[t] = 16384u + (8 * h + 4 * t + q4) * 256u + (((4 * fg + 2 * blk + (p4 >> 1)) ^ ((q4 << 2) | ((2 * h + t) & 3))) * 16u) + 8u * (p4 & 1);
#pragma unroll
  for (int kb = 0; kb < 7; ++kb) {
    if (w < 4) {
      if (kb == 0) asm volatile("s_waitcnt vmcnt(48)" ::: "memory");
      if (kb == 1) asm volatile("s_waitcnt vmcnt(40)" ::: "memory");
      if (kb == 2) asm volatile("s_waitcnt vmcnt(32)" ::: "memory");
      if (kb == 3) asm volatile("s_waitcnt vmcnt(24)" ::: "memory");
      if (kb == 4) asm volatile("s_waitcnt vmcnt(16)" ::: "memory");
      if (kb == 5) asm volatile("s_waitcnt vmcnt(8)" : "+v"(stg[0]), "+v"(stg[1]), "+v"(stg[2]), "+v"(stg[3]), "+v"(stg[4]), "+v"(stg[5]), "+v"(stg[6]), "+v"(stg[7])::"memory");
      if (kb == 6) asm volatile("s_waitcnt vmcnt(0)" : "+v"(stg[8]), "+v"(stg[9]), "+v"(stg[10]), "+v"(stg[11]), "+v"(stg[12]), "+v"(stg[13]), "+v"(stg[14]), "+v"(stg[15])::"memory");
      if (kb >= 5) {
#pragma unroll
        for (int i = 0; i < 4; ++i) {
          *LDSP(u4, sm + (kb - 5) * 32768u + (4 * w + i) * 1024u + lane * 16u) = stg[(kb - 5) * 8 + 2 * i];
          *LDSP(u4, sm + (kb - 5) * 32768u + 16384u + (4 * w + i) * 1024u + lane * 16u) = stg[(kb - 5) * 8 + 2 * i + 1];
        }
      }
      if (kb == 6) tag = flag_get(ws, 0) + 1;
    } else if (kb == 5) {
      poll(ws, OFF_KVF + (((b >> 5) * 4 + ((b & 31) >> 3)) * 32 + r31) * 8u, tag);
#pragma unroll
      for (int i = 0; i < 2; ++i) {
        const u32 o = OFF_KVR + (2 * b + i) * 4096u + (tid - 256) * 16u;
        rreg[i] = __builtin_amdgcn_raw_buffer_load_b128(rsW, o, 0, 16);
        rext[i] = __builtin_amdgcn_raw_buffer_load_b32(rsW, o + 16u, 0, 16);
      }
    }
    if (kb == 0) {
      asm volatile("" : "+v"(bias[0]), "+v"(bias[1]), "+v"(bias[2]), "+v"(bias[3])::"memory");
#pragma unroll
      for (int i = 0; i < 4; ++i) {
        acc0[4 * i] = bflo(bias[i][0]); acc0[4 * i + 1] = bfhi(bias[i][0]);
        acc0[4 * i + 2] = bflo(bias[i][1]); acc0[4 * i + 3] = bfhi(bias[i][1]);
      }
      acc1 = acc0;
    }
    bar();
    const u32 sb = sm + (kb % 5) * 32768u;
#pragma unroll
    for (int s = 0; s < 4; ++s) {
      b8 af = cat8(trread(sb + wa[0] + s * 4096u), trread(sb + wa[1] + s * 4096u));
      b8 x0 = *LDSP(b8, sb + xa0 + (((2 * s + h) ^ xk) * 16u));
      b8 x1 = *LDSP(b8, sb + xa0 + 4096u + (((2 * s + h) ^ xk) * 16u));
      acc0 = __builtin_amdgcn_mfma_f32_32x32x16_bf16(af, x0, acc0, 0, 0, 0);
      acc1 = __builtin_amdgcn_mfma_f32_32x32x16_bf16(af, x1, acc1, 0, 0, 0);
    }
  }
  STAMP(2);
  bar();
  if (w >= 4) {
    const int t8 = tid - 256;
#pragma unroll
    for (int i = 0; i < 2; ++i) {
      const u32 kb0 = sm + CV_K + (2 * i) * KST, kb1 = kb0 + KST;
      *LDSP(u4, kb0 + 64u + t8 * 16u) = kreg[i];
#pragma unroll
      for (int e = 0; e < 4; ++e) *LDSP(u32, kb1 + 60u + t8 * 16u + e * 4u) = kreg[i][e];
      const u32 rb0 = sm + CV_R + (2 * i) * RST, rb1 = rb0 + RST;
      *LDSP(u4, rb0 + t8 * 16u) = rreg[i];
      const u32 ex = t8 == 255 ? 0u : rext[i];
      u4 sh = {__builtin_amdgcn_alignbit(rreg[i][1], rreg[i][0], 16), __builtin_amdgcn_alignbit(rreg[i][2], rreg[i][1], 16),
               __builtin_amdgcn_alignbit(rreg[i][3], rreg[i][2], 16), __builtin_amdgcn_alignbit(ex, rreg[i][3], 16)};
      *LDSP(u4, rb1 + t8 * 16u) = sh;
    }
    const u4 z4 = {0u, 0u, 0u, 0u};
    if (t8 < 112) *LDSP(u4, sm + CV_R + (t8 / 28) * RST + 4096u + (t8 % 28) * 16u) = z4;
    else if (t8 >= 128 && t8 < 192) {
      const int u = t8 - 128, cpy = u >> 4, e = u & 15;
      if (!((cpy & 1) && e == 15)) *LDSP(u32, sm + CV_K + cpy * KST + e * 4u) = 0u;
    }
  }
  {
    const u32 ho = OFF_H + ((4 * mt + 2 * mh) * 112u + 8 * ft + 2 * fg) * 1024u + lane * 16u;
#pragma unroll
    for (int s = 0; s < 2; ++s) {
      u4 o;
#pragma unroll
      for (int e = 0; e < 4; ++e) o[e] = pack2(mish(acc0[8 * s + 2 * e]), mish(acc0[8 * s + 2 * e + 1]));
      __builtin_amdgcn_raw_buffer_store_b128(o, rsW, ho + s * 1024u, 0, 16);
    }
#pragma unroll
    for (int s = 0; s < 2; ++s) {
      u4 o;
#pragma unroll
      for (int e = 0; e < 4; ++e) o[e] = pack2(mish(acc1[8 * s + 2 * e]), mish(acc1[8 * s + 2 * e + 1]));
      __builtin_amdgcn_raw_buffer_store_b128(o, rsW, ho + (112u + s) * 1024u, 0, 16);
    }
  }
  const float dcoef = bflo((u32) * (const unsigned short*)(SSMD + (2 * b + (w >> 2)) * 2));
  STAMP(3);
  vm0();
  if (lane == 0) flag_set(ws, OFF_HF + ((2 * mt + mh) * 56 + 4 * ft + fg) * 8u, tag);
  const int kq = w & 3, m2 = w >> 2;
  const rsrc_t rsW2 = mkrs(W2, 1792u * 896u);
  const u32 w2o = (kq * 448 + 8 * m2 + (lane >> 3)) * 896u + cb * 128u + (((lane & 7) ^ (((lane >> 4) & 1) << 2)) * 16u);
#pragma unroll
  for (int s = 0; s < 14; ++s) glds16(sm + (kq * 14 + s) * 2048u + m2 * 1024u, w2o, rsW2, s * 16u * 896u);
  bar();
  STAMP(4);

  {
    const int ch = w >> 2, bq = w & 3;
    const int i16 = lane & 15, g4 = lane >> 4;
    const u32 aa = sm + CV_R + (2 * ch + 1 - (i16 & 1)) * RST + (2016 - 32 * (i16 >> 1) + 8 * g4) * 2u + bq * 128u;
    u32 ba = sm + CV_K + (2 * ch + 1 - (i16 & 1)) * KST + (2 * i16 + 2 * (i16 & 1) + 8 * g4) * 2u + bq * 128u;
    u32 ba2 = ba + 8u;
    asm volatile("" : "+v"(ba2));
    f4v cacc[8];
#pragma unroll
    for (int t = 0; t < 8; ++t) cacc[t] = (f4v){0.f, 0.f, 0.f, 0.f};
#pragma unroll
    for (int bb = 0; bb < 2; ++bb) {
      b8 A[8], B[8];
#pragma unroll
      for (int e = 0; e < 8; ++e) {
        A[e] = *LDSP(b8, aa + bb * 64u - e * 512u);
        B[e] = cat8(*LDSP(b4, ba + bb * 64u + e * 512u), *LDSP(b4, ba2 + bb * 64u + e * 512u));
      }
#pragma unroll
      for (int e = 0; e < 8; ++e)
#pragma unroll
        for (int a = 0; a + e < 8; ++a) cacc[a + e] = __builtin_amdgcn_mfma_f32_16x16x32_bf16(A[e], B[a], cacc[a + e], 0, 0, 0);
    }
    u32 kvp[4];
#pragma unroll
    for (int i = 0; i < 4; ++i) {
      const int te = 256 * (2 * bq + (i >> 1)) + 64 * g4 + 32 * (i & 1) + 2 * i16;
      kvp[i] = *LDSP(u32, sm + CV_R + 2 * ch * RST + (2046 - te) * 2u);
    }
    STAMP(5);
    bar();
    const u32 rb = sm + 114688u + ch * 24576u;
    red_w(bq, cacc, rb, lane);
    bar();
    f4v r0, r1;
    red_r(bq, cacc, rb, lane, r0, r1);
    u2 y0 = {pack2(r0[0] + bfhi(kvp[0]) * dcoef, r0[1] + bflo(kvp[0]) * dcoef), pack2(r0[2] + bfhi(kvp[1]) * dcoef, r0[3] + bflo(kvp[1]) * dcoef)};
    u2 y1 = {pack2(r1[0] + bfhi(kvp[2]) * dcoef, r1[1] + bflo(kvp[2]) * dcoef), pack2(r1[2] + bfhi(kvp[3]) * dcoef, r1[3] + bflo(kvp[3]) * dcoef)};
    const u32 yo = OFF_Y2 + (2 * b + ch) * 4096u + bq * 1024u + lane * 8u;
    __builtin_amdgcn_raw_buffer_store_b64(y0, rsW, yo, 0, 16);
    __builtin_amdgcn_raw_buffer_store_b64(y1, rsW, yo + 512u, 0, 16);
    vm0();
    if (lane == 0) flag_set(ws, OFF_YF + (bq * 448 + 2 * b + ch) * 8u, tag);
    bar();
  }
  STAMP(6);

  {
#pragma unroll
    for (int s = 14; s < 20; ++s) glds16(sm + 114688u + (kq * 6 + s - 14) * 2048u + m2 * 1024u, w2o, rsW2, s * 16u * 896u);
    poll(ws, OFF_HF + (tb * 56 + kq * 14 + (lane < 14 ? lane : 0)) * 8u, tag);
    STAMP(7);
    u4 hf[28];
    const u32 ho = OFF_H + ((2 * tb + m2) * 112u + 28 * kq) * 1024u + lane * 16u;
#pragma unroll
    for (int s = 0; s < 28; ++s) hf[s] = __builtin_amdgcn_raw_buffer_load_b128(rsW, ho + s * 1024u, 0, 16);
    const int d0 = 64 * cb + 16 * kq + 4 * h, mrow = 64 * tb + 32 * m2 + r31;
    u2 qv[2];
#pragma unroll
    for (int t = 0; t < 2; ++t) qv[t] = __builtin_amdgcn_raw_buffer_load_b64(rsQ, mrow * 896u + (d0 + 8 * t) * 2u, 0, 0);
    f16v c0, c1;
#pragma unroll
    for (int i = 0; i < 16; ++i) { c0[i] = 0.f; c1[i] = 0.f; }
    const u32 ta = (4 * h + q4) * 128u + (((2 * blk + (p4 >> 1)) ^ ((q4 >> 1) << 2)) * 16u) + 8u * (p4 & 1);
#pragma unroll
    for (int s = 0; s < 28; ++s) {
      if (s == 8) {
        bar();
#pragma unroll
        for (int s2 = 20; s2 < 28; ++s2) glds16(sm + (kq * 14 + s2 - 20) * 2048u + m2 * 1024u, w2o, rsW2, s2 * 16u * 896u);
      }
      if (s == 14) bar();
      if (s == 20) { vm0(); bar(); }
      const u32 sb = sm + (s < 14 ? (kq * 14 + s) * 2048u : s < 20 ? 114688u + (kq * 6 + s - 14) * 2048u : (kq * 14 + s - 20) * 2048u);
      b8 a0 = cat8(trread(sb + ta), trread(sb + ta + 1024u));
      b8 a1 = cat8(trread(sb + (ta ^ 64u)), trread(sb + (ta ^ 64u) + 1024u));
      b8 hb = __builtin_bit_cast(b8, hf[s]);
      c0 = __builtin_amdgcn_mfma_f32_32x32x16_bf16(a0, hb, c0, 0, 0, 0);
      c1 = __builtin_amdgcn_mfma_f32_32x32x16_bf16(a1, hb, c1, 0, 0, 0);
    }
    STAMP(8);
    poll(ws, OFF_YF + ((tb >> 3) * 448 + 64 * cb + lane) * 8u, tag);
    const u4 yv = __builtin_amdgcn_raw_buffer_load_b128(rsW, OFF_Y2 + (64 * cb + (tid >> 3)) * 4096u + tb * 128u + (tid & 7) * 16u, 0, 16);
    u64 gr;
    for (int it = 0; it < SPIN_LIM; ++it) {
      gr = flag_get(ws, OFF_G + mrow * 8u);
      if (__all((gr >> 16) == (tag & 0xffffffffffffull))) break;
      __builtin_amdgcn_s_sleep(1);
    }
    const float g = bflo((u32)gr & 0xffffu);
    f4v qd[8];
#pragma unroll
    for (int u = 0; u < 4; ++u) {
      qd[u] = (f4v){c0[4 * u], c0[4 * u + 1], c0[4 * u + 2], c0[4 * u + 3]};
      qd[4 + u] = (f4v){c1[4 * u], c1[4 * u + 1], c1[4 * u + 2], c1[4 * u + 3]};
    }
    bar();
    const u32 rb = sm + m2 * 24576u;
    red_w(kq, qd, rb, lane);
    {
      const u32 ya = sm + 49152u + (tid >> 3) * 136u + (tid & 7) * 8u;
      *LDSP(u2, ya) = (u2){yv[0], yv[2]};
      *LDSP(u2, ya + 64u) = (u2){yv[1], yv[3]};
    }
    bar();
    f4v r[2];
    red_r(kq, qd, rb, lane, r[0], r[1]);
    const rsrc_t rsO = mkrs(out, 2048u * 896u);
#pragma unroll
    for (int t = 0; t < 2; ++t) {
      u2 y2 = __builtin_bit_cast(u2, trread(sm + 49152u + (16 * kq + 8 * t + 4 * h + q4) * 136u + (32 * m2 + 16 * blk + 4 * p4) * 2u));
      float o0 = g * (bflo(qv[t][0]) * bflo(y2[0])) + (1.f - g) * r[t][0];
      float o1 = g * (bfhi(qv[t][0]) * bfhi(y2[0])) + (1.f - g) * r[t][1];
      float o2 = g * (bflo(qv[t][1]) * bflo(y2[1])) + (1.f - g) * r[t][2];
      float o3 = g * (bfhi(qv[t][1]) * bfhi(y2[1])) + (1.f - g) * r[t][3];
      u2 o = {pack2(o0, o1), pack2(o2, o3)};
      __builtin_amdgcn_raw_buffer_store_b64(o, rsO, mrow * 896u + (d0 + 8 * t) * 2u, 0, 0);
    }
  }
  STAMP(9);
}

extern "C" void kernel_launch(void* const* d_in, const int* in_sizes, int n_in, void* d_out, int out_size, void* d_ws, size_t ws_size,
                              hipStream_t stream) {
  fused_kernel<<<256, 512, 0, stream>>>((const char*)d_in[0], (const char*)d_in[1], (const char*)d_in[2], (const char*)d_in[3],
                                        (const char*)d_in[4], (const char*)d_in[5], (const char*)d_in[7], (const char*)d_in[8],
                                        (const char*)d_in[9], (const char*)d_in[10], (char*)d_out, (char*)d_ws);
}
```

```cpp
#include <hip/hip_runtime.h>

typedef unsigned int u32;
typedef unsigned long long u64;
typedef __attribute__((ext_vector_type(2))) __bf16 b2;
typedef __attribute__((ext_vector_type(4))) __bf16 b4;
typedef __attribute__((ext_vector_type(8))) __bf16 b8;
typedef __attribute__((ext_vector_type(2))) unsigned u2;
typedef __attribute__((ext_vector_type(4))) unsigned u4;
typedef __attribute__((ext_vector_type(2))) float f2v;
typedef __attribute__((ext_vector_type(4))) float f4v;
typedef __attribute__((ext_vector_type(16))) float f16v;
typedef __amdgpu_buffer_rsrc_t rsrc_t;
#define LDSP(T, a) ((__attribute__((address_space(3))) T*)(size_t)(a))
#define DI __device__ __forceinline__

#define OFF_KVF 1024u
#define OFF_G 16384u
#define OFF_HF 32768u
#define OFF_YF 49152u
#define OFF_KVR 65536u
#define OFF_Y2 (OFF_KVR + 448u * 4096u + 4096u)
#define OFF_H (OFF_Y2 + 448u * 4096u + 4096u)
#define WS_USED (OFF_H + 2048u * 1792u * 2u)
#define SPIN_LIM 400000

#define STAMP(i) do { } while (0)
#define STAMPDECL

DI float bflo(u32 v) { return __uint_as_float(v << 16); }
DI float bfhi(u32 v) { return __uint_as_float(v & 0xffff0000u); }
DI u32 pack2(float lo, float hi) {
  f2v f = {lo, hi};
  b2 r = __builtin_convertvector(f, b2);
  return __builtin_bit_cast(u32, r);
}
DI rsrc_t mkrs(const void* p, u32 bytes) { return __builtin_amdgcn_make_buffer_rsrc((void*)p, 0, bytes, 0x00020000); }
DI void glds16(u32 ldsaddr, u32 voff, rsrc_t rs, u32 soff) {
  asm volatile("s_mov_b32 m0, %0\n\ts_nop 0\n\tbuffer_load_dwordx4 %1, %2, %3 offen lds" ::"s"(ldsaddr), "v"(voff), "s"(rs), "s"(soff) : "memory");
}
DI u4 aload16(u32 voff, rsrc_t rs, u32 soff) {
  u4 r;
  asm volatile("buffer_load_dwordx4 %0, %1, %2, %3 offen" : "=v"(r) : "v"(voff), "s"(rs), "s"(soff) : "memory");
  return r;
}
DI u2 aload8(u32 voff, rsrc_t rs, u32 soff) {
  u2 r;
  asm volatile("buffer_load_dwordx2 %0, %1, %2, %3 offen" : "=v"(r) : "v"(voff), "s"(rs), "s"(soff) : "memory");
  return r;
}
DI void bar() { asm volatile("s_waitcnt lgkmcnt(0)\n\ts_barrier" ::: "memory"); }
DI void vm0() { asm volatile("s_waitcnt vmcnt(0)" ::: "memory"); }
DI void flag_set(char* ws, u32 off, u64 tag) {
  __hip_atomic_store((u64*)(ws + off), tag, __ATOMIC_RELAXED, __HIP_MEMORY_SCOPE_AGENT);
}
DI u64 flag_get(char* ws, u32 off) { return __hip_atomic_load((u64*)(ws + off), __ATOMIC_RELAXED, __HIP_MEMORY_SCOPE_AGENT); }
DI void poll(char* ws, u32 off, u64 tag) {
  for (int it = 0; it < SPIN_LIM; ++it) {
    u64 v = flag_get(ws, off);
    if (__all(v == tag)) break;
    __builtin_amdgcn_s_sleep(1);
  }
  asm volatile("" ::: "memory");
}
DI b8 cat8(b4 a, b4 c) { return __builtin_shufflevector(a, c, 0, 1, 2, 3, 4, 5, 6, 7); }
DI b4 trread(u32 a) { return __builtin_amdgcn_ds_read_tr16_b64_v4bf16(LDSP(b4, a)); }

DI float mish(float h) {
  float e = __builtin_amdgcn_exp2f(h * 1.44269504088896f);
  const float c = 0.70710678118654752f;
  float u = __builtin_fmaf(e, c, c);
  float r = __builtin_amdgcn_rcpf(__builtin_fmaf(u, u, 0.5f));
  return __builtin_fmaf(-h, r, h);
}

template <int Q>
DI void red_write(const f4v (&a)[8], u32 rb, int lane) {
#pragma unroll
  for (int o = 0; o < 4; ++o) {
    if (o == Q) continue;
    const int op = (o - Q - 1) & 3;
    *LDSP(f4v, rb + (Q * 6 + op * 2) * 1024 + lane * 16) = a[2 * o];
    *LDSP(f4v, rb + (Q * 6 + op * 2 + 1) * 1024 + lane * 16) = a[2 * o + 1];
  }
}
template <int Q>
DI void red_read(const f4v (&a)[8], u32 rb, int lane, f4v& r0, f4v& r1) {
  r0 = a[2 * Q];
  r1 = a[2 * Q + 1];
#pragma unroll
  for (int s = 0; s < 4; ++s) {
    if (s == Q) continue;
    const int op = (Q - s - 1) & 3;
    r0 += *LDSP(f4v, rb + (s * 6 + op * 2) * 1024 + lane * 16);
    r1 += *LDSP(f4v, rb + (s * 6 + op * 2 + 1) * 1024 + lane * 16);
  }
}
DI void red_w(int q, const f4v (&a)[8], u32 rb, int lane) {
  if (q == 0) red_write<0>(a, rb, lane);
  else if (q == 1) red_write<1>(a, rb, lane);
  else if (q == 2) red_write<2>(a, rb, lane);
  else red_write<3>(a, rb, lane);
}
DI void red_r(int q, const f4v (&a)[8], u32 rb, int lane, f4v& r0, f4v& r1) {
  if (q == 0) red_read<0>(a, rb, lane, r0, r1);
  else if (q == 1) red_read<1>(a, rb, lane, r0, r1);
  else if (q == 2) red_read<2>(a, rb, lane, r0, r1);
  else red_read<3>(a, rb, lane, r0, r1);
}

#define CV_K 0u
#define CV_R 16896u
#define KST 4224u
#define RST 4576u
#define W2S 65536u
#define W2E 49152u
#define SB0() __builtin_amdgcn_sched_barrier(0)
#define WAITV(n) asm volatile("s_waitcnt vmcnt(" #n ")" ::: "memory")
template <int N>
DI void waitv1(u4& a) { asm volatile("s_waitcnt vmcnt(%1)" : "+v"(a) : "n"(N) : "memory"); }
template <int OFF>
DI u4 lds128(u32 a) { u4 r; asm volatile("ds_read_b128 %0, %1 offset:%2" : "=v"(r) : "v"(a), "n"(OFF) : "memory"); return r; }
template <int OFF>
DI u2 lds64(u32 a) { u2 r; asm volatile("ds_read_b64 %0, %1 offset:%2" : "=v"(r) : "v"(a), "n"(OFF) : "memory"); return r; }
DI u4 aload16s(u32 voff, rsrc_t rs, u32 soff) {
  u4 r;
  asm volatile("buffer_load_dwordx4 %0, %1, %2, %3 offen sc1" : "=v"(r) : "v"(voff), "s"(rs), "s"(soff) : "memory");
  return r;
}
DI u2 aload8s(u32 voff, rsrc_t rs, u32 soff) {
  u2 r;
  asm volatile("buffer_load_dwordx2 %0, %1, %2, %3 offen sc1" : "=v"(r) : "v"(voff), "s"(rs), "s"(soff) : "memory");
  return r;
}
DI u64 mk64(u2 v) { return ((u64)v[1] << 32) | v[0]; }
DI b8 mkb8(u2 lo, u2 hi) { u4 t = {lo[0], lo[1], hi[0], hi[1]}; return __builtin_bit_cast(b8, t); }
struct CF { u4 A[8]; u2 L[8], H[8]; };
template <int BB, int I>
DI void cv_load(CF& f, u32 aa, u32 ba) {
  if constexpr (I == 0) f.A[0] = lds128<3584 + BB * 64>(aa);
  else if constexpr (I <= 8) { f.L[I - 1] = lds64<BB * 64 + (I - 1) * 512>(ba); f.H[I - 1] = lds64<BB * 64 + (I - 1) * 512 + 8>(ba); }
  else f.A[I - 8] = lds128<3584 + BB * 64 - (I - 8) * 512>(aa);
}
#define MF16(e, a) cacc[(a) + (e)] = __builtin_amdgcn_mfma_f32_16x16x32_bf16(__builtin_bit_cast(b8, f.A[e]), mkb8(f.L[a], f.H[a]), cacc[(a) + (e)], 0, 0, 0)
#define WAITL(n, ...) asm volatile("s_waitcnt lgkmcnt(" #n ")" : __VA_ARGS__::"memory")

__global__ __launch_bounds__(512, 2) void fused_kernel(const char* __restrict__ Q, const char* __restrict__ Kin, const char* __restrict__ V,
                                                       const char* __restrict__ SK, const char* __restrict__ SSMK,
                                                       const char* __restrict__ SSMD, const char* __restrict__ W1,
                                                       const char* __restrict__ B1, const char* __restrict__ W2,
                                                       const char* __restrict__ GW, char* __restrict__ out, char* __restrict__ ws) {
  __shared__ __attribute__((aligned(16))) char smem[163840];
  const u32 sm = (u32)(size_t)smem;
  const int tid = threadIdx.x, lane = tid & 63;
  const int w = __builtin_amdgcn_readfirstlane(tid >> 6);
  const rsrc_t rsQ = mkrs(Q, 2048u * 896u);
  const rsrc_t rsW = mkrs(ws, WS_USED);

  if (blockIdx.x < 32) {
    const u64 tag = flag_get(ws, 0) + 1;
    const int row0 = blockIdx.x * 64 + w * 8;
    const u32 lo = lane < 56 ? lane * 16u : 0x80000000u;
    u4 gw = __builtin_amdgcn_raw_buffer_load_b128(mkrs(GW, 896u), lo, 0, 0);
    u4 xr[8];
#pragma unroll
    for (int i = 0; i < 8; ++i) xr[i] = __builtin_amdgcn_raw_buffer_load_b128(rsQ, lo, (row0 + i) * 896u, 0);
    float mine = 0.f;
#pragma unroll
    for (int i = 0; i < 8; ++i) {
      float s = 0.f;
#pragma unroll
      for (int e = 0; e < 4; ++e) s += bflo(xr[i][e]) * bflo(gw[e]) + bfhi(xr[i][e]) * bfhi(gw[e]);
#pragma unroll
      for (int m = 1; m < 64; m <<= 1) s += __shfl_xor(s, m);
      if (lane == i) mine = s;
    }
    if (lane < 8) {
      float g = __builtin_amdgcn_rcpf(1.f + __builtin_amdgcn_exp2f(-1.44269504088896f * mine));
      u64 gr = (tag << 16) | (u64)(pack2(g, 0.f) & 0xffffu);
      flag_set(ws, OFF_G + (row0 + lane) * 8u, gr);
    }
    if (blockIdx.x == 0) {
      for (int i = 0; i < 4; ++i) {
        int f = tid + i * 512;
        poll(ws, OFF_HF + (f < 1792 ? f : 0) * 8u, tag);
      }
      for (int it = 0; it < SPIN_LIM; ++it) {
        u64 v = flag_get(ws, OFF_G + (tid & 255) * 64u);
        if (__all((v >> 16) == (tag & 0xffffffffffffull))) break;
        __builtin_amdgcn_s_sleep(2);
      }
      __syncthreads();
      if (tid == 0) flag_set(ws, 0, tag);
    }
    return;
  }

  const int b = blockIdx.x - 32;
  const int xcd = b & 7, idx = b >> 3;
  const int mt = 2 * xcd + idx / 14, ft = idx % 14;
  const int tb = 4 * xcd + idx / 7, cb = idx % 7;
  const int h = lane >> 5, r31 = lane & 31;
  const int blk = (lane >> 4) & 1, q4 = (lane & 15) >> 2, p4 = lane & 3;
  STAMPDECL;
  STAMP(0);

  const int fg = w & 3, mh = w >> 2;
  const int kq = w & 3, m2 = w >> 2;
  u4 stg[16];
  u2 bias[4], tg;
  u4 kreg[2], rreg[2];
  u32 rext[2];
  const rsrc_t rsW1 = mkrs(W1, 448u * 3584u);
  const rsrc_t rsW2 = mkrs(W2, 1792u * 896u);
  const u32 w2l = (lane >> 3) * 896u + cb * 128u + (((lane & 7) ^ (((lane >> 4) & 1) << 2)) * 16u);
  if (w >= 4) __builtin_amdgcn_s_setprio(3);
  tg = aload8s(0u, rsW, 0u);
  {
    const rsrc_t rsB1 = mkrs(B1, 3584u);
#pragma unroll
    for (int i = 0; i < 4; ++i) bias[i] = aload8((ft * 128 + fg * 32 + 8 * i + 4 * h) * 2u, rsB1, 0);
  }
  if (w < 4) {
    u32 xo[4], wo[4];
#pragma unroll
    for (int i = 0; i < 4; ++i) {
      int row = 32 * w + 8 * i + (lane >> 3);
      xo[i] = (mt * 128 + row) * 896u + (((lane & 7) ^ ((row >> 1) & 7)) * 16u);
      int rw = 16 * w + 4 * i + (lane >> 4);
      wo[i] = rw * 3584u + ft * 256u + (((lane & 15) ^ (((rw & 3) << 2) | ((rw >> 2) & 3))) * 16u);
    }
#pragma unroll
    for (int kb = 0; kb < 5; ++kb) {
#pragma unroll
      for (int i = 0; i < 4; ++i) {
        glds16(sm + kb * 32768u + (4 * w + i) * 1024u, xo[i], rsQ, kb * 128u);
        glds16(sm + kb * 32768u + 16384u + (4 * w + i) * 1024u, wo[i], rsW1, kb * 64u * 3584u);
      }
    }
#pragma unroll
    for (int kb = 5; kb < 7; ++kb) {
#pragma unroll
      for (int i = 0; i < 4; ++i) {
        stg[(kb - 5) * 8 + 2 * i] = aload16(xo[i], rsQ, kb * 128u);
        stg[(kb - 5) * 8 + 2 * i + 1] = aload16(wo[i], rsW1, kb * 64u * 3584u);
      }
    }
    WAITV(48);
  } else {
    const int pw = w - 4;
    const int oct = lane & 7, cp = 8 * pw + (lane >> 3);
    const int c = 64 * cb + 2 * cp, t0 = 64 * tb + 8 * oct;
    const rsrc_t rsK = mkrs(Kin, 2048u * 896u), rsV = mkrs(V, 2048u * 896u);
    u32 kk[11], vv[8];
    const u32 o0 = t0 * 896u + c * 2u;
    u4 sk = __builtin_amdgcn_raw_buffer_load_b128(mkrs(SK, 3584u), c * 8u, 0, 0);
#pragma unroll
    for (int i = 0; i < 3; ++i) kk[i] = __builtin_amdgcn_raw_buffer_load_b32(rsK, t0 > 0 ? o0 - (3 - i) * 896u : 0x80000000u, 0, 0);
#pragma unroll
    for (int i = 0; i < 8; ++i) {
      kk[3 + i] = __builtin_amdgcn_raw_buffer_load_b32(rsK, o0 + i * 896u, 0, 0);
      vv[i] = __builtin_amdgcn_raw_buffer_load_b32(rsV, o0 + i * 896u, 0, 0);
    }
    SB0();
    float a0[8], a1[8];
#pragma unroll
    for (int r = 0; r < 8; ++r) {
      float s0 = bflo(kk[r]) * bfhi(sk[1]) + bflo(kk[r + 1]) * bflo(sk[1]) + bflo(kk[r + 2]) * bfhi(sk[0]) + bflo(kk[r + 3]) * bflo(sk[0]);
      float s1 = bfhi(kk[r]) * bfhi(sk[3]) + bfhi(kk[r + 1]) * bflo(sk[3]) + bfhi(kk[r + 2]) * bfhi(sk[2]) + bfhi(kk[r + 3]) * bflo(sk[2]);
      a0[r] = s0 * bflo(vv[r]);
      a1[r] = s1 * bfhi(vv[r]);
    }
    u4 o0v = {pack2(a0[7], a0[6]), pack2(a0[5], a0[4]), pack2(a0[3], a0[2]), pack2(a0[1], a0[0])};
    u4 o1v = {pack2(a1[7], a1[6]), pack2(a1[5], a1[4]), pack2(a1[3], a1[2]), pack2(a1[1], a1[0])};
    const u32 so = OFF_KVR + c * 4096u + (2040 - t0) * 2u;
    __builtin_amdgcn_raw_buffer_store_b128(o0v, rsW, so, 0, 16);
    __builtin_amdgcn_raw_buffer_store_b128(o1v, rsW, so + 4096u, 0, 16);
    __builtin_amdgcn_s_setprio(0);
  }
  asm volatile("" : "+v"(bias[0]), "+v"(bias[1]), "+v"(bias[2]), "+v"(bias[3]), "+v"(tg)::"memory");
  const u64 tag = mk64(tg) + 1;
  STAMP(1);

  f16v acc0, acc1;
#pragma unroll
  for (int i = 0; i < 4; ++i) {
    acc0[4 * i] = bflo(bias[i][0]); acc0[4 * i + 1] = bfhi(bias[i][0]);
    acc0[4 * i + 2] = bflo(bias[i][1]); acc0[4 * i + 3] = bfhi(bias[i][1]);
  }
  acc1 = acc0;
  u32 xa[4], wa[2];
#pragma unroll
  for (int s = 0; s < 4; ++s) xa[s] = sm + (64 * mh + r31) * 128u + (((2 * s + h) ^ ((r31 >> 1) & 7)) * 16u);
#pragma unroll
  for (int t = 0; t < 2; ++t)
    wa[t] = sm + 16384u + (8 * h + 4 * t + q4) * 256u + (((4 * fg + 2 * blk + (p4 >> 1)) ^ ((q4 << 2) | ((2 * h + t) & 3))) * 16u) + 8u * (p4 & 1);
#pragma unroll
  for (int kb = 0; kb < 7; ++kb) {
    if (w < 4) {
      if (kb == 1) WAITV(40);
      if (kb == 2) WAITV(32);
      if (kb == 3) WAITV(24);
      if (kb == 4) WAITV(24);
      if (kb == 5) asm volatile("s_waitcnt vmcnt(24)" : "+v"(stg[0]), "+v"(stg[1]), "+v"(stg[2]), "+v"(stg[3]), "+v"(stg[4]), "+v"(stg[5]), "+v"(stg[6]), "+v"(stg[7])::"memory");
      if (kb == 6) asm volatile("s_waitcnt vmcnt(24)" : "+v"(stg[8]), "+v"(stg[9]), "+v"(stg[10]), "+v"(stg[11]), "+v"(stg[12]), "+v"(stg[13]), "+v"(stg[14]), "+v"(stg[15])::"memory");
      if (kb >= 5) {
#pragma unroll
        for (int i = 0; i < 4; ++i) {
          *LDSP(u4, sm + (kb - 5) * 32768u + (4 * w + i) * 1024u + lane * 16u) = stg[(kb - 5) * 8 + 2 * i];
          *LDSP(u4, sm + (kb - 5) * 32768u + 16384u + (4 * w + i) * 1024u + lane * 16u) = stg[(kb - 5) * 8 + 2 * i + 1];
        }
      }
    } else {
      if (kb == 1) {
        vm0();
        if (lane == 0) flag_set(ws, OFF_KVF + ((cb * 4 + w - 4) * 32 + tb) * 8u, tag);
        const rsrc_t rsSK = mkrs(SSMK, 448u * 4096u);
#pragma unroll
        for (int i = 0; i < 2; ++i) kreg[i] = aload16((tid - 256) * 16u, rsSK, (2 * b + i) * 4096u);
      }
      if (kb == 5) {
        poll(ws, OFF_KVF + (((b >> 5) * 4 + ((b & 31) >> 3)) * 32 + r31) * 8u, tag);
#pragma unroll
        for (int i = 0; i < 2; ++i) {
          const u32 o = OFF_KVR + (2 * b + i) * 4096u + (tid - 256) * 16u;
          rreg[i] = aload16s(o, rsW, 0);
          asm volatile("buffer_load_dword %0, %1, %2, 0 offen offset:16 sc1" : "=v"(rext[i]) : "v"(o), "s"(rsW) : "memory");
        }
      }
    }
    bar();
    STAMP(10 + kb);
    if (kb >= 3 && kb <= 5 && w < 4) {
#pragma unroll
      for (int i = 0; i < 8; ++i) {
        const int n = (kb - 3) * 32 + w * 8 + i, ks = n >> 1;
        glds16(sm + W2S + n * 1024u, w2l, rsW2, ((ks / 12) * 448 + (ks % 12) * 16 + (n & 1) * 8) * 896u);
      }
    }
    const u32 so = (kb % 5) * 32768u;
    b8 af[4], x0[4], x1[4];
#pragma unroll
    for (int s = 0; s < 4; ++s) {
      af[s] = cat8(trread(wa[0] + so + s * 4096u), trread(wa[1] + so + s * 4096u));
      x0[s] = *LDSP(b8, xa[s] + so);
      x1[s] = *LDSP(b8, xa[s] + so + 4096u);
    }
    SB0();
#pragma unroll
    for (int s = 0; s < 4; ++s) {
      acc0 = __builtin_amdgcn_mfma_f32_32x32x16_bf16(af[s], x0[s], acc0, 0, 0, 0);
      acc1 = __builtin_amdgcn_mfma_f32_32x32x16_bf16(af[s], x1[s], acc1, 0, 0, 0);
    }
    SB0();
  }
  STAMP(2);
  bar();
  if (w >= 4) {
    asm volatile("s_waitcnt vmcnt(0)" : "+v"(kreg[0]), "+v"(kreg[1]), "+v"(rreg[0]), "+v"(rreg[1]), "+v"(rext[0]), "+v"(rext[1])::"memory");
    const int t8 = tid - 256;
#pragma unroll
    for (int i = 0; i < 2; ++i) {
      const u32 kb0 = sm + CV_K + (2 * i) * KST, kb1 = kb0 + KST;
      *LDSP(u4, kb0 + 64u + t8 * 16u) = kreg[i];
#pragma unroll
      for (int e = 0; e < 4; ++e) *LDSP(u32, kb1 + 60u + t8 * 16u + e * 4u) = kreg[i][e];
      const u32 rb0 = sm + CV_R + (2 * i) * RST, rb1 = rb0 + RST;
      *LDSP(u4, rb0 + t8 * 16u) = rreg[i];
      const u32 ex = t8 == 255 ? 0u : rext[i];
      u4 sh = {__builtin_amdgcn_alignbit(rreg[i][1], rreg[i][0], 16), __builtin_amdgcn_alignbit(rreg[i][2], rreg[i][1], 16),
               __builtin_amdgcn_alignbit(rreg[i][3], rreg[i][2], 16), __builtin_amdgcn_alignbit(ex, rreg[i][3], 16)};
      *LDSP(u4, rb1 + t8 * 16u) = sh;
    }
    const u4 z4 = {0u, 0u, 0u, 0u};
    if (t8 < 112) *LDSP(u4, sm + CV_R + (t8 / 28) * RST + 4096u + (t8 % 28) * 16u) = z4;
    else if (t8 >= 128 && t8 < 192) {
      const int u = t8 - 128, cpy = u >> 4, e = u & 15;
      if (!((cpy & 1) && e == 15)) *LDSP(u32, sm + CV_K + cpy * KST + e * 4u) = 0u;
    }
  }
  {
    const u32 ho = OFF_H + ((4 * mt + 2 * mh) * 112u + 8 * ft + 2 * fg) * 1024u + lane * 16u;
#pragma unroll
    for (int s = 0; s < 2; ++s) {
      u4 o;
#pragma unroll
      for (int e = 0; e < 4; ++e) o[e] = pack2(mish(acc0[8 * s + 2 * e]), mish(acc0[8 * s + 2 * e + 1]));
      __builtin_amdgcn_raw_buffer_store_b128(o, rsW, ho + s * 1024u, 0, 16);
    }
#pragma unroll
    for (int s = 0; s < 2; ++s) {
      u4 o;
#pragma unroll
      for (int e = 0; e < 4; ++e) o[e] = pack2(mish(acc1[8 * s + 2 * e]), mish(acc1[8 * s + 2 * e + 1]));
      __builtin_amdgcn_raw_buffer_store_b128(o, rsW, ho + (112u + s) * 1024u, 0, 16);
    }
  }
  const u32 dpair = *(const u32*)(SSMD + b * 4);
  const float dcoef = (w >> 2) ? bfhi(dpair) : bflo(dpair);
  STAMP(3);
  WAITV(4);
  bar();
  const u32 ta = sm + (4 * h + q4) * 128u + (((2 * blk + (p4 >> 1)) ^ ((q4 >> 1) << 2)) * 16u) + 8u * (p4 & 1);
  const u32 tc = sm + (4 * h + q4) * 128u + (((2 * blk + (p4 >> 1) + 4) ^ ((q4 >> 1) << 2)) * 16u) + 8u * (p4 & 1);
  b8 wr[24];
  {
    const u32 o = W2S + kq * 24576u;
#pragma unroll
    for (int s = 0; s < 12; ++s) {
      wr[2 * s] = cat8(trread(ta + o + s * 2048u), trread(ta + o + s * 2048u + 1024u));
      wr[2 * s + 1] = cat8(trread(tc + o + s * 2048u), trread(tc + o + s * 2048u + 1024u));
    }
  }
  bar();
  const u32 w2s = (kq * 448 + m2 * 8) * 896u;
#pragma unroll
  for (int s = 12; s < 24; ++s) glds16(sm + W2S + (kq * 12 + s - 12) * 2048u + m2 * 1024u, w2l, rsW2, w2s + s * 16u * 896u);
#pragma unroll
  for (int s = 24; s < 26; ++s) glds16(sm + W2E + (kq * 2 + s - 24) * 2048u + m2 * 1024u, w2l, rsW2, w2s + s * 16u * 896u);
  STAMP(4);

  u4 hf[28];
  const u32 ho = OFF_H + ((2 * tb + m2) * 112u + 28 * kq) * 1024u + lane * 16u;
  {
    const int ch = w >> 2, bq = w & 3;
    const int i16 = lane & 15, g4 = lane >> 4;
    const u32 aa = sm + CV_R + (2 * ch + 1 - (i16 & 1)) * RST + (2016 - 32 * (i16 >> 1) + 8 * g4) * 2u + bq * 128u - 3584u;
    const u32 ba = sm + CV_K + (2 * ch + 1 - (i16 & 1)) * KST + (2 * i16 + 2 * (i16 & 1) + 8 * g4) * 2u + bq * 128u;
    f4v cacc[8];
#pragma unroll
    for (int t = 0; t < 8; ++t) cacc[t] = (f4v){0.f, 0.f, 0.f, 0.f};
    CF f, n;
    cv_load<0, 0>(f, aa, ba); cv_load<0, 1>(f, aa, ba); cv_load<0, 2>(f, aa, ba); cv_load<0, 3>(f, aa, ba); cv_load<0, 4>(f, aa, ba);
    cv_load<0, 5>(f, aa, ba); cv_load<0, 6>(f, aa, ba); cv_load<0, 7>(f, aa, ba); cv_load<0, 8>(f, aa, ba);
    cv_load<0, 9>(f, aa, ba); cv_load<0, 10>(f, aa, ba); cv_load<0, 11>(f, aa, ba); cv_load<0, 12>(f, aa, ba);
    cv_load<0, 13>(f, aa, ba); cv_load<0, 14>(f, aa, ba); cv_load<0, 15>(f, aa, ba);
    WAITL(15, "+v"(f.A[0]), "+v"(f.L[0]), "+v"(f.H[0]), "+v"(f.L[1]), "+v"(f.H[1]), "+v"(f.L[2]), "+v"(f.H[2]), "+v"(f.L[3]), "+v"(f.H[3]));
    MF16(0, 0); MF16(0, 1); MF16(0, 2); MF16(0, 3);
    SB0();
    WAITL(6, "+v"(f.A[1]), "+v"(f.L[4]), "+v"(f.H[4]), "+v"(f.L[5]), "+v"(f.H[5]), "+v"(f.L[6]), "+v"(f.H[6]), "+v"(f.L[7]), "+v"(f.H[7]));
    MF16(0, 4); MF16(0, 5); MF16(0, 6); MF16(0, 7);
    MF16(1, 0); MF16(1, 1); MF16(1, 2); MF16(1, 3); MF16(1, 4); MF16(1, 5); MF16(1, 6);
    SB0();
    WAITL(0, "+v"(f.A[2]), "+v"(f.A[3]), "+v"(f.A[4]), "+v"(f.A[5]), "+v"(f.A[6]), "+v"(f.A[7]));
    cv_load<1, 0>(n, aa, ba); cv_load<1, 1>(n, aa, ba); cv_load<1, 2>(n, aa, ba); cv_load<1, 3>(n, aa, ba);
    MF16(2, 0); MF16(2, 1); MF16(2, 2); MF16(2, 3); MF16(2, 4); MF16(2, 5);
    SB0();
    cv_load<1, 4>(n, aa, ba); cv_load<1, 5>(n, aa, ba);
    MF16(3, 0); MF16(3, 1); MF16(3, 2); MF16(3, 3); MF16(3, 4);
    SB0();
    cv_load<1, 6>(n, aa, ba); cv_load<1, 7>(n, aa, ba);
    MF16(4, 0); MF16(4, 1); MF16(4, 2); MF16(4, 3);
    SB0();
    cv_load<1, 8>(n, aa, ba); cv_load<1, 9>(n, aa, ba);
    MF16(5, 0); MF16(5, 1); MF16(5, 2);
    SB0();
    cv_load<1, 10>(n, aa, ba); cv_load<1, 11>(n, aa, ba);
    MF16(6, 0); MF16(6, 1); MF16(7, 0);
    SB0();
    cv_load<1, 12>(n, aa, ba); cv_load<1, 13>(n, aa, ba); cv_load<1, 14>(n, aa, ba); cv_load<1, 15>(n, aa, ba);
    WAITV(14);
    if (lane == 0) flag_set(ws, OFF_HF + ((2 * mt + mh) * 56 + 4 * ft + fg) * 8u, tag);
    f = n;
    WAITL(6, "+v"(f.A[0]), "+v"(f.L[0]), "+v"(f.H[0]), "+v"(f.L[1]), "+v"(f.H[1]), "+v"(f.L[2]), "+v"(f.H[2]), "+v"(f.L[3]), "+v"(f.H[3]),
          "+v"(f.A[1]), "+v"(f.L[4]), "+v"(f.H[4]), "+v"(f.L[5]), "+v"(f.H[5]), "+v"(f.L[6]), "+v"(f.H[6]), "+v"(f.L[7]), "+v"(f.H[7]));
    MF16(0, 0); MF16(0, 1); MF16(0, 2); MF16(0, 3); MF16(0, 4); MF16(0, 5); MF16(0, 6); MF16(0, 7);
    MF16(1, 0); MF16(1, 1); MF16(1, 2); MF16(1, 3); MF16(1, 4); MF16(1, 5); MF16(1, 6);
    SB0();
    WAITL(0, "+v"(f.A[2]), "+v"(f.A[3]), "+v"(f.A[4]), "+v"(f.A[5]), "+v"(f.A[6]), "+v"(f.A[7]));
    u32 kvp[4];
#pragma unroll
    for (int i = 0; i < 4; ++i) {
      const int te = 256 * (2 * bq + (i >> 1)) + 64 * g4 + 32 * (i & 1) + 2 * i16;
      kvp[i] = *LDSP(u32, sm + CV_R + 2 * ch * RST + (2046 - te) * 2u);
    }
    u2 hfl = aload8s(OFF_HF + (tb * 56 + kq * 14 + (lane < 14 ? lane : 0)) * 8u, rsW, 0);
    SB0();
    MF16(2, 0); MF16(2, 1); MF16(2, 2); MF16(2, 3); MF16(2, 4); MF16(2, 5);
    MF16(3, 0); MF16(3, 1); MF16(3, 2); MF16(3, 3); MF16(3, 4);
    MF16(4, 0); MF16(4, 1); MF16(4, 2); MF16(4, 3);
    MF16(5, 0); MF16(5, 1); MF16(5, 2);
    MF16(6, 0); MF16(6, 1); MF16(7, 0);
    SB0();
    STAMP(5);
    asm volatile("s_waitcnt vmcnt(0)" : "+v"(hfl)::"memory");
    if (!__all(mk64(hfl) == tag)) poll(ws, OFF_HF + (tb * 56 + kq * 14 + (lane < 14 ? lane : 0)) * 8u, tag);
    STAMP(7);
#pragma unroll
    for (int s = 0; s < 12; ++s) hf[s] = aload16s(ho, rsW, s * 1024u);
    bar();
    const u32 rb = sm + ch * 24576u;
    red_w(bq, cacc, rb, lane);
    bar();
    f4v r0, r1;
    red_r(bq, cacc, rb, lane, r0, r1);
    u2 y0 = {pack2(r0[0] + bfhi(kvp[0]) * dcoef, r0[1] + bflo(kvp[0]) * dcoef), pack2(r0[2] + bfhi(kvp[1]) * dcoef, r0[3] + bflo(kvp[1]) * dcoef)};
    u2 y1 = {pack2(r1[0] + bfhi(kvp[2]) * dcoef, r1[1] + bflo(kvp[2]) * dcoef), pack2(r1[2] + bfhi(kvp[3]) * dcoef, r1[3] + bflo(kvp[3]) * dcoef)};
    const u32 yo = OFF_Y2 + (2 * b + ch) * 4096u + bq * 1024u + lane * 8u;
    __builtin_amdgcn_raw_buffer_store_b64(y0, rsW, yo, 0, 16);
    __builtin_amdgcn_raw_buffer_store_b64(y1, rsW, yo + 512u, 0, 16);
    bar();
  }
  STAMP(6);

  {
#pragma unroll
    for (int i = 0; i < 4; ++i) glds16(sm + w * 6144u + i * 1024u, w2l, rsW2, (kq * 448 + 416 + i * 8) * 896u);
#pragma unroll
    for (int s = 12; s < 20; ++s) hf[s] = aload16s(ho, rsW, s * 1024u);
    const int d0 = 64 * cb + 16 * kq + 4 * h, mrow = 64 * tb + 32 * m2 + r31;
    u2 qv[2], gr, yfl;
    u4 yv[2];
    f16v c0, c1;
#pragma unroll
    for (int i = 0; i < 16; ++i) { c0[i] = 0.f; c1[i] = 0.f; }
    b8 a0, a1, n0, n1;
    const u32 yfo = OFF_YF + ((tb >> 3) * 448 + 64 * cb + 16 * kq + (lane & 15)) * 8u;
#define W2LD(s, A0, A1) do { const u32 o_ = (s) < 24 ? W2S + (kq * 12 + (s) - 12) * 2048u : (s) < 26 ? W2E + (kq * 2 + (s) - 24) * 2048u : w * 6144u + ((s) - 26) * 2048u; \
      A0 = cat8(trread(ta + o_), trread(ta + o_ + 1024u)); A1 = cat8(trread(tc + o_), trread(tc + o_ + 1024u)); } while (0)
#define G2STEP(s, N) do { waitv1<N>(hf[s]); \
      if ((s) >= 11 && (s) < 27) W2LD((s) + 1, n0, n1); \
      SB0(); \
      if ((s) < 12) { a0 = wr[2 * ((s) % 12)]; a1 = wr[2 * ((s) % 12) + 1]; } \
      c0 = __builtin_amdgcn_mfma_f32_32x32x16_bf16(a0, __builtin_bit_cast(b8, hf[s]), c0, 0, 0, 0); \
      c1 = __builtin_amdgcn_mfma_f32_32x32x16_bf16(a1, __builtin_bit_cast(b8, hf[s]), c1, 0, 0, 0); \
      a0 = n0; a1 = n1; SB0(); } while (0)
    G2STEP(0, 25); G2STEP(1, 24); G2STEP(2, 23); G2STEP(3, 22);
#pragma unroll
    for (int s = 20; s < 28; ++s) hf[s] = aload16s(ho, rsW, s * 1024u);
    qv[0] = aload8(mrow * 896u + d0 * 2u, rsQ, 0);
    qv[1] = aload8(mrow * 896u + d0 * 2u, rsQ, 16);
    gr = aload8s(OFF_G + mrow * 8u, rsW, 0);
    G2STEP(4, 32); G2STEP(5, 31); G2STEP(6, 30); G2STEP(7, 29); G2STEP(8, 28); G2STEP(9, 27); G2STEP(10, 26); G2STEP(11, 25);
    WAITV(23);
    if (lane == 0) flag_set(ws, OFF_YF + ((w & 3) * 448 + 2 * b + (w >> 2)) * 8u, tag);
    G2STEP(12, 19); G2STEP(13, 18); G2STEP(14, 17); G2STEP(15, 16); G2STEP(16, 15); G2STEP(17, 14); G2STEP(18, 13); G2STEP(19, 12);
    G2STEP(20, 11); G2STEP(21, 10);
    yfl = aload8s(yfo, rsW, 0);
    G2STEP(22, 10); G2STEP(23, 9); G2STEP(24, 8); G2STEP(25, 7);
    asm volatile("s_waitcnt vmcnt(0)" : "+v"(yfl), "+v"(qv[0]), "+v"(qv[1]), "+v"(gr)::"memory");
    STAMP(8);
    if (!__all(mk64(yfl) == tag)) poll(ws, yfo, tag);
#pragma unroll
    for (int i = 0; i < 2; ++i) yv[i] = aload16s(OFF_Y2 + (64 * cb + 16 * kq + 8 * i + (lane >> 3)) * 4096u + tb * 128u + (lane & 7) * 16u, rsW, 0);
    G2STEP(26, 2); G2STEP(27, 2);
    u64 g64 = mk64(gr);
    if (!__all((g64 >> 16) == (tag & 0xffffffffffffull))) {
      for (int it = 0; it < SPIN_LIM; ++it) {
        g64 = flag_get(ws, OFF_G + mrow * 8u);
        if (__all((g64 >> 16) == (tag & 0xffffffffffffull))) break;
        __builtin_amdgcn_s_sleep(1);
      }
    }
    const float g = bflo((u32)g64 & 0xffffu);
    f4v qd[8];
#pragma unroll
    for (int u = 0; u < 4; ++u) {
      qd[u] = (f4v){c0[4 * u], c0[4 * u + 1], c0[4 * u + 2], c0[4 * u + 3]};
      qd[4 + u] = (f4v){c1[4 * u], c1[4 * u + 1], c1[4 * u + 2], c1[4 * u + 3]};
    }
    const u32 rb = sm + m2 * 24576u;
    red_w(kq, qd, rb, lane);
    bar();
    const u32 yb = sm + W2S + w * 2048u;
    asm volatile("s_waitcnt vmcnt(0)" : "+v"(yv[0]), "+v"(yv[1])::"memory");
#pragma unroll
    for (int i = 0; i < 2; ++i)
      *LDSP(u2, yb + (8 * i + (lane >> 3)) * 72u + (lane & 7) * 8u) = m2 ? (u2){yv[i][1], yv[i][3]} : (u2){yv[i][0], yv[i][2]};
    f4v r[2];
    red_r(kq, qd, rb, lane, r[0], r[1]);
    const rsrc_t rsO = mkrs(out, 2048u * 896u);
#pragma unroll
    for (int t = 0; t < 2; ++t) {
      u2 y2 = __builtin_bit_cast(u2, trread(yb + (8 * t + 4 * h + q4) * 72u + (16 * blk + 4 * p4) * 2u));
      float o0 = g * (bflo(qv[t][0]) * bflo(y2[0])) + (1.f - g) * r[t][0];
      float o1 = g * (bfhi(qv[t][0]) * bfhi(y2[0])) + (1.f - g) * r[t][1];
      float o2 = g * (bflo(qv[t][1]) * bflo(y2[1])) + (1.f - g) * r[t][2];
      float o3 = g * (bfhi(qv[t][1]) * bfhi(y2[1])) + (1.f - g) * r[t][3];
      u2 o = {pack2(o0, o1), pack2(o2, o3)};
      __builtin_amdgcn_raw_buffer_store_b64(o, rsO, mrow * 896u + (d0 + 8 * t) * 2u, 0, 0);
    }
  }
  STAMP(9);
}

extern "C" void kernel_launch(void* const* d_in, const int* in_sizes, int n_in, void* d_out, int out_size, void* d_ws, size_t ws_size,
                              hipStream_t stream) {
  fused_kernel<<<256, 512, 0, stream>>>((const char*)d_in[0], (const char*)d_in[1], (const char*)d_in[2], (const char*)d_in[3],
                                        (const char*)d_in[4], (const char*)d_in[5], (const char*)d_in[7], (const char*)d_in[8],
                                        (const char*)d_in[9], (const char*)d_in[10], (char*)d_out, (char*)d_ws);
}
```

```cpp
#include <hip/hip_runtime.h>

typedef unsigned int u32;
typedef unsigned long long u64;
typedef __attribute__((ext_vector_type(2))) __bf16 b2;
typedef __attribute__((ext_vector_type(4))) __bf16 b4;
typedef __attribute__((ext_vector_type(8))) __bf16 b8;
typedef __attribute__((ext_vector_type(2))) unsigned u2;
typedef __attribute__((ext_vector_type(4))) unsigned u4;
typedef __attribute__((ext_vector_type(2))) float f2v;
typedef __attribute__((ext_vector_type(4))) float f4v;
typedef __attribute__((ext_vector_type(16))) float f16v;
typedef __amdgpu_buffer_rsrc_t rsrc_t;
#define LDSP(T, a) ((__attribute__((address_space(3))) T*)(size_t)(a))
#define DI __device__ __forceinline__

#define OFF_G 16384u
#define OFF_HF 32768u
#define OFF_KVR 65536u
#define OFF_Y2 (OFF_KVR + 448u * 8192u + 4096u)
#define OFF_H (OFF_Y2 + 448u * 8192u + 4096u)
#define WS_USED (OFF_H + 2048u * 1792u * 2u)
#define SPIN_LIM 400000
#define HACQ 0
#define MAP 2
#define DIAG 0
#define ROT 1
#define NH0 16
#define LA 4
#define HLOAD aload16s

#define STAMP(i) do { } while (0)
#define STAMPDECL

DI float bflo(u32 v) { return __uint_as_float(v << 16); }
DI float bfhi(u32 v) { return __uint_as_float(v & 0xffff0000u); }
DI u32 pack2(float lo, float hi) {
  f2v f = {lo, hi};
  b2 r = __builtin_convertvector(f, b2);
  return __builtin_bit_cast(u32, r);
}
DI rsrc_t mkrs(const void* p, u32 bytes) { return __builtin_amdgcn_make_buffer_rsrc((void*)p, 0, bytes, 0x00020000); }
DI void glds16(u32 ldsaddr, u32 voff, rsrc_t rs, u32 soff) {
  asm volatile("s_mov_b32 m0, %0\n\ts_nop 0\n\tbuffer_load_dwordx4 %1, %2, %3 offen lds" ::"s"(ldsaddr), "v"(voff), "s"(rs), "s"(soff) : "memory");
}
DI u4 aload16(u32 voff, rsrc_t rs, u32 soff) {
  u4 r;
  asm volatile("buffer_load_dwordx4 %0, %1, %2, %3 offen" : "=v"(r) : "v"(voff), "s"(rs), "s"(soff) : "memory");
  return r;
}
DI u2 aload8(u32 voff, rsrc_t rs, u32 soff) {
  u2 r;
  asm volatile("buffer_load_dwordx2 %0, %1, %2, %3 offen" : "=v"(r) : "v"(voff), "s"(rs), "s"(soff) : "memory");
  return r;
}
DI void bar() { asm volatile("s_waitcnt lgkmcnt(0)\n\ts_barrier" ::: "memory"); }
DI void vm0() { asm volatile("s_waitcnt vmcnt(0)" ::: "memory"); }
DI void flag_set(char* ws, u32 off, u64 tag) {
  __hip_atomic_store((u64*)(ws + off), tag, __ATOMIC_RELAXED, __HIP_MEMORY_SCOPE_AGENT);
}
DI u64 flag_get(char* ws, u32 off) { return __hip_atomic_load((u64*)(ws + off), __ATOMIC_RELAXED, __HIP_MEMORY_SCOPE_AGENT); }
DI void poll(char* ws, u32 off, u64 tag) {
  for (int it = 0; it < SPIN_LIM; ++it) {
    u64 v = flag_get(ws, off);
    if (__all(v == tag)) break;
    __builtin_amdgcn_s_sleep(1);
  }
  asm volatile("" ::: "memory");
}
DI b8 cat8(b4 a, b4 c) { return __builtin_shufflevector(a, c, 0, 1, 2, 3, 4, 5, 6, 7); }
DI b4 trread(u32 a) { return __builtin_amdgcn_ds_read_tr16_b64_v4bf16(LDSP(b4, a)); }

DI u32 mish2(float h0, float h1) {
  const f2v h = {h0, h1};
  const f2v t = h * 1.44269504088896f;
  const f2v e = {__builtin_amdgcn_exp2f(t[0]), __builtin_amdgcn_exp2f(t[1])};
  const f2v c = {0.70710678118654752f, 0.70710678118654752f}, hf = {0.5f, 0.5f};
  const f2v u = __builtin_elementwise_fma(e, c, c);
  const f2v d = __builtin_elementwise_fma(u, u, hf);
  const f2v r = {__builtin_amdgcn_rcpf(d[0]), __builtin_amdgcn_rcpf(d[1])};
  const f2v o = __builtin_elementwise_fma(-h, r, h);
  return pack2(o[0], o[1]);
}
DI u32 aload4(u32 voff, rsrc_t rs, u32 soff) {
  u32 r;
  asm volatile("buffer_load_dword %0, %1, %2, %3 offen" : "=v"(r) : "v"(voff), "s"(rs), "s"(soff) : "memory");
  return r;
}

template <int Q>
DI void red_write(const f4v (&a)[8], u32 rb, int lane) {
#pragma unroll
  for (int o = 0; o < 4; ++o) {
    if (o == Q) continue;
    const int op = (o - Q - 1) & 3;
    *LDSP(f4v, rb + (Q * 6 + op * 2) * 1024 + lane * 16) = a[2 * o];
    *LDSP(f4v, rb + (Q * 6 + op * 2 + 1) * 1024 + lane * 16) = a[2 * o + 1];
  }
}
template <int Q>
DI void red_read(const f4v (&a)[8], u32 rb, int lane, f4v& r0, f4v& r1) {
  r0 = a[2 * Q];
  r1 = a[2 * Q + 1];
#pragma unroll
  for (int s = 0; s < 4; ++s) {
    if (s == Q) continue;
    const int op = (Q - s - 1) & 3;
    r0 += *LDSP(f4v, rb + (s * 6 + op * 2) * 1024 + lane * 16);
    r1 += *LDSP(f4v, rb + (s * 6 + op * 2 + 1) * 1024 + lane * 16);
  }
}
DI void red_w(int q, const f4v (&a)[8], u32 rb, int lane) {
  if (q == 0) red_write<0>(a, rb, lane);
  else if (q == 1) red_write<1>(a, rb, lane);
  else if (q == 2) red_write<2>(a, rb, lane);
  else red_write<3>(a, rb, lane);
}
DI void red_r(int q, const f4v (&a)[8], u32 rb, int lane, f4v& r0, f4v& r1) {
  if (q == 0) red_read<0>(a, rb, lane, r0, r1);
  else if (q == 1) red_read<1>(a, rb, lane, r0, r1);
  else if (q == 2) red_read<2>(a, rb, lane, r0, r1);
  else red_read<3>(a, rb, lane, r0, r1);
}

#define CV_K 0u
#define CV_R 16896u
#define KST 4224u
#define RST 4576u
#define W2S 65536u
#define W2E 49152u
#define SB0() __builtin_amdgcn_sched_barrier(0)
#define WAITV(n) asm volatile("s_waitcnt vmcnt(" #n ")" ::: "memory")
template <int N>
DI void waitv() { asm volatile("s_waitcnt vmcnt(%0)" ::"n"(N) : "memory"); }
template <int N>
DI void waitv1(u4& a) { asm volatile("s_waitcnt vmcnt(%1)" : "+v"(a) : "n"(N) : "memory"); }
template <int OFF>
DI u4 lds128(u32 a) { u4 r; asm volatile("ds_read_b128 %0, %1 offset:%2" : "=v"(r) : "v"(a), "n"(OFF) : "memory"); return r; }
template <int OFF>
DI u2 lds64(u32 a) { u2 r; asm volatile("ds_read_b64 %0, %1 offset:%2" : "=v"(r) : "v"(a), "n"(OFF) : "memory"); return r; }
DI u4 aload16s(u32 voff, rsrc_t rs, u32 soff) {
  u4 r;
  asm volatile("buffer_load_dwordx4 %0, %1, %2, %3 offen sc1" : "=v"(r) : "v"(voff), "s"(rs), "s"(soff) : "memory");
  return r;
}
DI u2 aload8s(u32 voff, rsrc_t rs, u32 soff) {
  u2 r;
  asm volatile("buffer_load_dwordx2 %0, %1, %2, %3 offen sc1" : "=v"(r) : "v"(voff), "s"(rs), "s"(soff) : "memory");
  return r;
}
DI u64 mk64(u2 v) { return ((u64)v[1] << 32) | v[0]; }
DI b8 mkb8(u2 lo, u2 hi) { u4 t = {lo[0], lo[1], hi[0], hi[1]}; return __builtin_bit_cast(b8, t); }
struct CF { u4 A[8]; u2 L[8], H[8]; };
template <int BB, int I>
DI void cv_load(CF& f, u32 aa, u32 ba) {
  if constexpr (I == 0) f.A[0] = lds128<3584 + BB * 64>(aa);
  else if constexpr (I <= 8) { f.L[I - 1] = lds64<BB * 64 + (I - 1) * 512>(ba); f.H[I - 1] = lds64<BB * 64 + (I - 1) * 512 + 8>(ba); }
  else f.A[I - 8] = lds128<3584 + BB * 64 - (I - 8) * 512>(aa);
}
#define MF16(e, a) cacc[(a) + (e)] = __builtin_amdgcn_mfma_f32_16x16x32_bf16(__builtin_bit_cast(b8, f.A[e]), mkb8(f.L[a], f.H[a]), cacc[(a) + (e)], 0, 0, 0)
#define WAITL(n, ...) asm volatile("s_waitcnt lgkmcnt(" #n ")" : __VA_ARGS__::"memory")

__global__ __launch_bounds__(512, 2) void fused_kernel(const char* __restrict__ Q, const char* __restrict__ Kin, const char* __restrict__ V,
                                                       const char* __restrict__ SK, const char* __restrict__ SSMK,
                                                       const char* __restrict__ SSMD, const char* __restrict__ W1,
                                                       const char* __restrict__ B1, const char* __restrict__ W2,
                                                       const char* __restrict__ GW, char* __restrict__ out, char* __restrict__ ws) {
  __shared__ __attribute__((aligned(16))) char smem[163840];
  const u32 sm = (u32)(size_t)smem;
  const int tid = threadIdx.x, lane = tid & 63;
  const int w = __builtin_amdgcn_readfirstlane(tid >> 6);
  const rsrc_t rsQ = mkrs(Q, 2048u * 896u);
  const rsrc_t rsW = mkrs(ws, WS_USED);

  if (blockIdx.x < 32) {
    const u64 tag = flag_get(ws, 0) + 1;
    const int row0 = blockIdx.x * 64 + w * 8;
    const u32 lo = lane < 56 ? lane * 16u : 0x80000000u;
    u4 gw = __builtin_amdgcn_raw_buffer_load_b128(mkrs(GW, 896u), lo, 0, 0);
    u4 xr[8];
#pragma unroll
    for (int i = 0; i < 8; ++i) xr[i] = __builtin_amdgcn_raw_buffer_load_b128(rsQ, lo, (row0 + i) * 896u, 0);
    float mine = 0.f;
#pragma unroll
    for (int i = 0; i < 8; ++i) {
      float s = 0.f;
#pragma unroll
      for (int e = 0; e < 4; ++e) s += bflo(xr[i][e]) * bflo(gw[e]) + bfhi(xr[i][e]) * bfhi(gw[e]);
#pragma unroll
      for (int m = 1; m < 64; m <<= 1) s += __shfl_xor(s, m);
      if (lane == i) mine = s;
    }
    if (lane < 8) {
      float g = __builtin_amdgcn_rcpf(1.f + __builtin_amdgcn_exp2f(-1.44269504088896f * mine));
      u64 gr = (tag << 16) | (u64)(pack2(g, 0.f) & 0xffffu);
      flag_set(ws, OFF_G + (row0 + lane) * 8u, gr);
    }
    if (blockIdx.x == 0) {
      for (int i = 0; i < 4; ++i) {
        int f = tid + i * 512;
        poll(ws, OFF_HF + (f < 1792 ? f : 0) * 8u, tag);
      }
      for (int it = 0; it < SPIN_LIM; ++it) {
        u64 v = flag_get(ws, OFF_G + (tid & 255) * 64u);
        if (__all((v >> 16) == (tag & 0xffffffffffffull))) break;
        __builtin_amdgcn_s_sleep(2);
      }
      __syncthreads();
      if (tid == 0) flag_set(ws, 0, tag);
    }
    return;
  }

  const int b = blockIdx.x - 32;
  const int xcd = b & 7, idx = b >> 3;
  const int mt = 4 * (xcd >> 1) + idx / 7, ft = 7 * (xcd & 1) + idx % 7;
  const int rot1 = ROT ? (idx % 7 + 2 * (idx / 7)) % 7 : 0;
  const int tb = 4 * xcd + idx / 7, cb = idx % 7;
  const int rot2 = ROT ? idx : 0;
#define KAP(it) ((it) + rot1 >= 7 ? (it) + rot1 - 7 : (it) + rot1)
#define SIG(s) ((s) + rot2 >= 28 ? (s) + rot2 - 28 : (s) + rot2)
  const int h = lane >> 5, r31 = lane & 31;
  const int blk = (lane >> 4) & 1, q4 = (lane & 15) >> 2, p4 = lane & 3;
  STAMPDECL;
  STAMP(0);

  const int fg = w & 3, mh = w >> 2;
  const int kq = w & 3, m2 = w >> 2;
  const int t8 = tid & 255;
  u2 bias[4], tg;
  u4 kreg, rr0, rr1;
  const rsrc_t rsW1 = mkrs(W1, 448u * 3584u);
  const rsrc_t rsW2 = mkrs(W2, 1792u * 896u);
  const u32 w2l = (lane >> 3) * 896u + cb * 128u + (((lane & 7) ^ (((lane >> 4) & 1) << 2)) * 16u);
  u32 xo[2], wo[2];
#pragma unroll
  for (int i = 0; i < 2; ++i) {
    int row = 16 * w + 8 * i + (lane >> 3);
    xo[i] = (mt * 128 + row) * 896u + (((lane & 7) ^ ((row >> 1) & 7)) * 16u);
    int rw = 8 * w + 4 * i + (lane >> 4);
    wo[i] = rw * 3584u + ft * 256u + (((lane & 15) ^ (((rw & 3) << 2) | ((rw >> 2) & 3))) * 16u);
  }
#define PART(p) do { const int it_ = (p) >> 2, i_ = (p) & 3; if (it_ < 7) { \
      if (i_ & 1) glds16(sm + (it_ % 5) * 32768u + 16384u + (2 * w + (i_ >> 1)) * 1024u, wo[i_ >> 1], rsW1, KAP(it_) * 64u * 3584u); \
      else glds16(sm + (it_ % 5) * 32768u + (2 * w + (i_ >> 1)) * 1024u, xo[i_ >> 1], rsQ, KAP(it_) * 128u); } \
    else { const int s_ = (it_ - 7) * 4 + i_; \
      glds16(sm + W2S + (s_ * 4 + kq) * 2048u + m2 * 1024u, w2l, rsW2, w2s + SIG(s_) * 16u * 896u); } } while (0)
#define WB(s) glds16(sm + W2S + (((s) - 12) * 4 + kq) * 2048u + m2 * 1024u, w2l, rsW2, w2s + SIG(s) * 16u * 896u)
#define WR(s) do { const u32 o_ = W2S + ((s) * 4 + kq) * 2048u; \
    wr[2 * (s)] = cat8(trread(ta + o_), trread(ta + o_ + 1024u)); wr[2 * (s) + 1] = cat8(trread(tc + o_), trread(tc + o_ + 1024u)); } while (0)
  const u32 w2s = (kq * 448 + m2 * 8) * 896u;
  const u32 ta = sm + (4 * h + q4) * 128u + (((2 * blk + (p4 >> 1)) ^ ((q4 >> 1) << 2)) * 16u) + 8u * (p4 & 1);
  const u32 tc = sm + (4 * h + q4) * 128u + (((2 * blk + (p4 >> 1) + 4) ^ ((q4 >> 1) << 2)) * 16u) + 8u * (p4 & 1);
  b8 wr[24];
#define WAIT2(a, c) asm volatile("s_waitcnt vmcnt(" #a ")\n\ts_cmp_gt_u32 %0, 3\n\ts_cbranch_scc1 1f\n\ts_waitcnt vmcnt(" #c ")\n1:" ::"s"(w) : "scc", "memory")
  PART(0); PART(1); PART(2); PART(3);
  tg = aload8s(0u, rsW, 0u);
  {
    const rsrc_t rsB1 = mkrs(B1, 3584u);
#pragma unroll
    for (int i = 0; i < 4; ++i) bias[i] = aload8((ft * 128 + fg * 32 + 8 * i + 4 * h) * 2u, rsB1, 0);
  }
  const rsrc_t rsSK = mkrs(SSMK, 448u * 4096u);
  const u32 dpair = *(const u32*)(SSMD + b * 4);
  const rsrc_t rsO = mkrs(out, 2048u * 896u);
  u2 kk[7], vv[4];
  u4 sk[2];
  const int pc = 64 * cb + 4 * (lane & 15), pt0 = 64 * tb + 16 * (w & 3) + 4 * (lane >> 4);
  if (w >= 4) {
    const rsrc_t rsK = mkrs(Kin, 2048u * 896u), rsV = mkrs(V, 2048u * 896u);
    const u32 o0 = pt0 * 896u + pc * 2u;
    const u32 om = pt0 > 0 ? o0 - 3 * 896u : 0x80000000u;
    sk[0] = aload16(pc * 8u, mkrs(SK, 3584u), 0);
    sk[1] = aload16(pc * 8u, mkrs(SK, 3584u), 16);
#pragma unroll
    for (int i = 0; i < 3; ++i) kk[i] = aload8(om, rsK, i * 896u);
#pragma unroll
    for (int i = 0; i < 4; ++i) {
      kk[3 + i] = aload8(o0, rsK, i * 896u);
      vv[i] = aload8(o0, rsV, i * 896u);
    }
  }
  PART(4); PART(5); PART(6); PART(7);
  asm volatile("" ::"s"(dpair), "s"(rsO), "s"(rsW2), "s"(rsSK));
  WAIT2(17, 4);
  asm volatile("" : "+v"(bias[0]), "+v"(bias[1]), "+v"(bias[2]), "+v"(bias[3]), "+v"(tg)::"memory");
  const u64 tag = mk64(tg) + 1;
  const u32 tag32 = (u32)tag;
  STAMP(1);

  f16v acc0, acc1;
#pragma unroll
  for (int i = 0; i < 4; ++i) {
    acc0[4 * i] = bflo(bias[i][0]); acc0[4 * i + 1] = bfhi(bias[i][0]);
    acc0[4 * i + 2] = bflo(bias[i][1]); acc0[4 * i + 3] = bfhi(bias[i][1]);
  }
  acc1 = acc0;
  u32 xa[4], wa[2];
#pragma unroll
  for (int s = 0; s < 4; ++s) xa[s] = sm + (64 * mh + r31) * 128u + (((2 * s + h) ^ ((r31 >> 1) & 7)) * 16u);
#pragma unroll
  for (int t = 0; t < 2; ++t)
    wa[t] = sm + 16384u + (8 * h + 4 * t + q4) * 256u + (((4 * fg + 2 * blk + (p4 >> 1)) ^ ((q4 << 2) | ((2 * h + t) & 3))) * 16u) + 8u * (p4 & 1);
  const u32 kvo = OFF_KVR + (2 * b + mh) * 8192u + t8 * 16u;
  b8 af[28], x0[28], x1[28];
#define G1LD(j) do { const u32 so_ = (((j) >> 2) % 5) * 32768u; \
    af[j] = cat8(trread(wa[0] + so_ + ((j) & 3) * 4096u), trread(wa[1] + so_ + ((j) & 3) * 4096u)); \
    x0[j] = *LDSP(b8, xa[(j) & 3] + so_); x1[j] = *LDSP(b8, xa[(j) & 3] + so_ + 4096u); } while (0)
  bar();
  STAMP(10);
  G1LD(0);
  G1LD(1);
#pragma unroll
  for (int j = 0; j < 28; ++j) {
    if ((j & 3) == 2) {
      const int kb = (j + 2) >> 2;
      if (kb == 1) WAITV(6);
      if (kb >= 2 && kb <= 3) WAIT2(14, 10);
      if (kb == 4) WAIT2(15, 11);
      if (kb == 5) WAITV(11);
      if (kb == 6) WAITV(12);
      if (kb == 7) WAITV(4);
      bar();
      if (kb < 7) STAMP(10 + kb);
    }
    if (j + 2 < 28 && DIAG != 1) G1LD(j + 2);
    SB0();
    if (DIAG != 1 || j < 2) {
    acc0 = __builtin_amdgcn_mfma_f32_32x32x16_bf16(af[j], x0[j], acc0, 0, 0, 0);
    acc1 = __builtin_amdgcn_mfma_f32_32x32x16_bf16(af[j], x1[j], acc1, 0, 0, 0);
    }
    SB0();
    if (j == 10) kreg = aload16(t8 * 16u, rsSK, (2 * b + mh) * 4096u);
    if (j == 18) { rr0 = aload16s(kvo, rsW, 0); rr1 = aload16s(kvo, rsW, 4096); }
    if (j < 4) { PART(8 + 3 * j); PART(9 + 3 * j); PART(10 + 3 * j); }
    else if (j < 24) PART(j + 16);
    if (j >= 26) glds16(sm + W2E + (kq * 2 + j - 26) * 2048u + m2 * 1024u, w2l, rsW2, (kq * 448 + m2 * 8 + SIG(j - 2) * 16u) * 896u);
    SB0();
    if (j == 3 && w >= 4) {
      asm volatile("s_waitcnt vmcnt(%13)" : "+v"(sk[0]), "+v"(sk[1]), "+v"(kk[0]), "+v"(kk[1]), "+v"(kk[2]), "+v"(kk[3]), "+v"(kk[4]), "+v"(kk[5]), "+v"(kk[6]),
                   "+v"(vv[0]), "+v"(vv[1]), "+v"(vv[2]), "+v"(vv[3]) : "n"(16) : "memory");
      const u32 so = OFF_KVR + pc * 8192u + (2044 - pt0) * 4u;
#pragma unroll
      for (int c = 0; c < 4; ++c) {
        const u32 s01 = sk[c >> 1][2 * (c & 1)], s23 = sk[c >> 1][2 * (c & 1) + 1];
        float kf[7], a[4];
#pragma unroll
        for (int r = 0; r < 7; ++r) kf[r] = (c & 1) ? bfhi(kk[r][c >> 1]) : bflo(kk[r][c >> 1]);
#pragma unroll
        for (int r = 0; r < 4; ++r) {
          float sv = kf[r] * bfhi(s23) + kf[r + 1] * bflo(s23) + kf[r + 2] * bfhi(s01) + kf[r + 3] * bflo(s01);
          a[r] = sv * ((c & 1) ? bfhi(vv[r][c >> 1]) : bflo(vv[r][c >> 1]));
        }
        __builtin_amdgcn_raw_buffer_store_b128((u4){pack2(a[3], a[2]), tag32, pack2(a[1], a[0]), tag32}, rsW, so + c * 8192u, 0, 16);
      }
      SB0();
    }
  }
  STAMP(2);
  WR(0); WR(1); WR(2); WR(3); WR(4); WR(5); WR(6); WR(7);
  SB0();
  {
    const u32 ho = OFF_H + ((4 * mt + 2 * mh) * 112u + 8 * ft + 2 * fg) * 1024u + lane * 16u;
#pragma unroll
    for (int s = 0; s < 2; ++s) {
      u4 o;
#pragma unroll
      for (int e = 0; e < 4; ++e) o[e] = mish2(acc0[8 * s + 2 * e], acc0[8 * s + 2 * e + 1]);
      __builtin_amdgcn_raw_buffer_store_b128(o, rsW, ho + s * 1024u, 0, 16);
    }
#pragma unroll
    for (int s = 0; s < 2; ++s) {
      u4 o;
#pragma unroll
      for (int e = 0; e < 4; ++e) o[e] = mish2(acc1[8 * s + 2 * e], acc1[8 * s + 2 * e + 1]);
      __builtin_amdgcn_raw_buffer_store_b128(o, rsW, ho + (112u + s) * 1024u, 0, 16);
    }
  }
  const float dcoef = (w >> 2) ? bfhi(dpair) : bflo(dpair);
  STAMP(3);
  {
    asm volatile("s_waitcnt vmcnt(6)" : "+v"(kreg), "+v"(rr0), "+v"(rr1)::"memory");
    for (int it = 0; it < SPIN_LIM; ++it) {
      if (__all(rr0[1] == tag32 && rr0[3] == tag32 && rr1[1] == tag32 && rr1[3] == tag32)) break;
      __builtin_amdgcn_s_sleep(1);
      rr0 = aload16s(kvo, rsW, 0); rr1 = aload16s(kvo, rsW, 4096);
      asm volatile("s_waitcnt vmcnt(0)" : "+v"(rr0), "+v"(rr1)::"memory");
    }
    const u32 kb0 = sm + CV_K + (2 * mh) * KST, kb1 = kb0 + KST;
    *LDSP(u4, kb0 + 64u + t8 * 16u) = kreg;
#pragma unroll
    for (int e = 0; e < 4; ++e) *LDSP(u32, kb1 + 60u + t8 * 16u + e * 4u) = kreg[e];
    const u32 rb0 = sm + CV_R + (2 * mh) * RST, rb1 = rb0 + RST;
#pragma unroll
    for (int i = 0; i < 2; ++i) {
      const u32 e01 = i ? rr1[0] : rr0[0], e23 = i ? rr1[2] : rr0[2];
      const u32 o = t8 * 8u + i * 2048u;
      *LDSP(u2, rb0 + o) = (u2){e01, e23};
      *LDSP(unsigned short, rb1 + o - 2u) = (unsigned short)e01;
      *LDSP(u32, rb1 + o) = __builtin_amdgcn_alignbit(e23, e01, 16);
      *LDSP(unsigned short, rb1 + o + 4u) = (unsigned short)(e23 >> 16);
    }
    if (t8 == 255) *LDSP(unsigned short, rb1 + 4094u) = 0;
    const u4 z4 = {0u, 0u, 0u, 0u};
    if (tid < 112) *LDSP(u4, sm + CV_R + (tid / 28) * RST + 4096u + (tid % 28) * 16u) = z4;
    else if (tid >= 256 && tid < 320) {
      const int u = tid - 256, cpy = u >> 4, e = u & 15;
      if (!((cpy & 1) && e == 15)) *LDSP(u32, sm + CV_K + cpy * KST + e * 4u) = 0u;
    }
  }
  bar();
  STAMP(4);

  u4 hf[28];
  const u32 ho = OFF_H + ((2 * tb + m2) * 112u + 28 * kq) * 1024u + lane * 16u;
  {
    const int ch = w >> 2, bq = w & 3;
    const int i16 = lane & 15, g4 = lane >> 4;
    const u32 aa = sm + CV_R + (2 * ch + 1 - (i16 & 1)) * RST + (2016 - 32 * (i16 >> 1) + 8 * g4) * 2u + bq * 128u - 3584u;
    const u32 ba = sm + CV_K + (2 * ch + 1 - (i16 & 1)) * KST + (2 * i16 + 2 * (i16 & 1) + 8 * g4) * 2u + bq * 128u;
    f4v cacc[8];
#pragma unroll
    for (int t = 0; t < 8; ++t) cacc[t] = (f4v){0.f, 0.f, 0.f, 0.f};
    CF f, n;
    cv_load<0, 0>(f, aa, ba); cv_load<0, 1>(f, aa, ba); cv_load<0, 2>(f, aa, ba); cv_load<0, 3>(f, aa, ba); cv_load<0, 4>(f, aa, ba);
    cv_load<0, 5>(f, aa, ba); cv_load<0, 6>(f, aa, ba); cv_load<0, 7>(f, aa, ba); cv_load<0, 8>(f, aa, ba);
    cv_load<0, 9>(f, aa, ba); cv_load<0, 10>(f, aa, ba); cv_load<0, 11>(f, aa, ba); cv_load<0, 12>(f, aa, ba);
    cv_load<0, 13>(f, aa, ba); cv_load<0, 14>(f, aa, ba); cv_load<0, 15>(f, aa, ba);
    WAITL(15, "+v"(f.A[0]), "+v"(f.L[0]), "+v"(f.H[0]), "+v"(f.L[1]), "+v"(f.H[1]), "+v"(f.L[2]), "+v"(f.H[2]), "+v"(f.L[3]), "+v"(f.H[3]));
    MF16(0, 0); MF16(0, 1); MF16(0, 2); MF16(0, 3);
    SB0();
    WB(12);
    WAITL(6, "+v"(f.A[1]), "+v"(f.L[4]), "+v"(f.H[4]), "+v"(f.L[5]), "+v"(f.H[5]), "+v"(f.L[6]), "+v"(f.H[6]), "+v"(f.L[7]), "+v"(f.H[7]));
    MF16(0, 4); MF16(0, 5); MF16(0, 6); MF16(0, 7); SB0(); WB(13);
    MF16(1, 0); MF16(1, 1); MF16(1, 2); MF16(1, 3); SB0(); WB(14); MF16(1, 4); MF16(1, 5); MF16(1, 6);
    SB0();
    WAITL(0, "+v"(f.A[2]), "+v"(f.A[3]), "+v"(f.A[4]), "+v"(f.A[5]), "+v"(f.A[6]), "+v"(f.A[7]));
    cv_load<1, 0>(n, aa, ba); cv_load<1, 1>(n, aa, ba); cv_load<1, 2>(n, aa, ba); cv_load<1, 3>(n, aa, ba);
    WB(15); WR(8);
    MF16(2, 0); MF16(2, 1); MF16(2, 2); MF16(2, 3); MF16(2, 4); MF16(2, 5);
    SB0();
    cv_load<1, 4>(n, aa, ba); cv_load<1, 5>(n, aa, ba);
    WB(16); WR(9);
    MF16(3, 0); MF16(3, 1); MF16(3, 2); MF16(3, 3); MF16(3, 4);
    SB0();
    cv_load<1, 6>(n, aa, ba); cv_load<1, 7>(n, aa, ba);
    WB(17); WR(10);
    MF16(4, 0); MF16(4, 1); MF16(4, 2); MF16(4, 3);
    SB0();
    cv_load<1, 8>(n, aa, ba); cv_load<1, 9>(n, aa, ba);
    WB(18); WR(11);
    MF16(5, 0); MF16(5, 1); MF16(5, 2);
    SB0();
    cv_load<1, 10>(n, aa, ba); cv_load<1, 11>(n, aa, ba);
    WB(19);
    MF16(6, 0); MF16(6, 1); MF16(7, 0);
    SB0();
    cv_load<1, 12>(n, aa, ba); cv_load<1, 13>(n, aa, ba); cv_load<1, 14>(n, aa, ba); cv_load<1, 15>(n, aa, ba);
    WAITV(8);
    STAMP(17);
    if (lane == 0) flag_set(ws, OFF_HF + ((2 * mt + mh) * 56 + 4 * ft + fg) * 8u, tag);
    f = n;
    bar();
    WAITL(6, "+v"(f.A[0]), "+v"(f.L[0]), "+v"(f.H[0]), "+v"(f.L[1]), "+v"(f.H[1]), "+v"(f.L[2]), "+v"(f.H[2]), "+v"(f.L[3]), "+v"(f.H[3]),
          "+v"(f.A[1]), "+v"(f.L[4]), "+v"(f.H[4]), "+v"(f.L[5]), "+v"(f.H[5]), "+v"(f.L[6]), "+v"(f.H[6]), "+v"(f.L[7]), "+v"(f.H[7]));
    MF16(0, 0); MF16(0, 1); MF16(0, 2); SB0(); WB(20); MF16(0, 3); MF16(0, 4); MF16(0, 5); SB0(); WB(21); MF16(0, 6); MF16(0, 7);
    MF16(1, 0); SB0(); WB(22); MF16(1, 1); MF16(1, 2); MF16(1, 3); SB0(); WB(23); MF16(1, 4); MF16(1, 5); MF16(1, 6);
    SB0();
    WAITL(0, "+v"(f.A[2]), "+v"(f.A[3]), "+v"(f.A[4]), "+v"(f.A[5]), "+v"(f.A[6]), "+v"(f.A[7]));
    u32 kvp[4];
#pragma unroll
    for (int i = 0; i < 4; ++i) {
      const int te = 256 * (2 * bq + (i >> 1)) + 64 * g4 + 32 * (i & 1) + 2 * i16;
      kvp[i] = *LDSP(u32, sm + CV_R + 2 * ch * RST + (2046 - te) * 2u);
    }
    const u32 hfo = OFF_HF + (tb * 56 + kq * 14 + (lane < 14 ? lane : 0)) * 8u;
    SB0();
    MF16(2, 0); MF16(2, 1); MF16(2, 2); MF16(2, 3); MF16(2, 4); MF16(2, 5);
    MF16(3, 0); MF16(3, 1); MF16(3, 2); MF16(3, 3); MF16(3, 4);
    MF16(4, 0); MF16(4, 1); MF16(4, 2); MF16(4, 3);
    SB0();
    u2 hfl = aload8s(hfo, rsW, 0);
    bar();
    MF16(5, 0); MF16(5, 1); MF16(5, 2);
    MF16(6, 0); MF16(6, 1); MF16(7, 0);
    SB0();
    STAMP(5);
    const u32 rb = sm + ch * 24576u;
    red_w(bq, cacc, rb, lane);
    asm volatile("s_waitcnt vmcnt(0)" : "+v"(hfl)::"memory");
    if (!__all(mk64(hfl) == tag)) poll(ws, hfo, tag);
    STAMP(7);
#pragma unroll
    for (int s = 0; s < NH0; ++s) hf[s] = HLOAD(ho, rsW, SIG(s) * 1024u);
    bar();
    f4v r0, r1;
    red_r(bq, cacc, rb, lane, r0, r1);
    const u32 yo = OFF_Y2 + (2 * b + ch) * 8192u + bq * 2048u + g4 * 256u + i16 * 8u;
    __builtin_amdgcn_raw_buffer_store_b64((u2){pack2(r0[0] + bfhi(kvp[0]) * dcoef, r0[1] + bflo(kvp[0]) * dcoef), tag32}, rsW, yo, 0, 16);
    __builtin_amdgcn_raw_buffer_store_b64((u2){pack2(r0[2] + bfhi(kvp[1]) * dcoef, r0[3] + bflo(kvp[1]) * dcoef), tag32}, rsW, yo + 128u, 0, 16);
    __builtin_amdgcn_raw_buffer_store_b64((u2){pack2(r1[0] + bfhi(kvp[2]) * dcoef, r1[1] + bflo(kvp[2]) * dcoef), tag32}, rsW, yo + 1024u, 0, 16);
    __builtin_amdgcn_raw_buffer_store_b64((u2){pack2(r1[2] + bfhi(kvp[3]) * dcoef, r1[3] + bflo(kvp[3]) * dcoef), tag32}, rsW, yo + 1152u, 0, 16);
    bar();
  }
  STAMP(6);

  {
#pragma unroll
    for (int i = 0; i < 4; ++i) glds16(sm + w * 6144u + i * 1024u, w2l, rsW2, (kq * 448 + SIG(26 + (i >> 1)) * 16 + (i & 1) * 8) * 896u);
#pragma unroll
    for (int s = NH0; s < 20; ++s) hf[s] = HLOAD(ho, rsW, SIG(s) * 1024u);
    const int d0 = 64 * cb + 16 * kq + 4 * h, mrow = 64 * tb + 32 * m2 + r31;
    u2 qv[2], gr;
    u4 yv[2];
    f16v c0, c1;
#pragma unroll
    for (int i = 0; i < 16; ++i) { c0[i] = 0.f; c1[i] = 0.f; }
    b8 a0, a1, n0, n1;
    const u32 yo = OFF_Y2 + (64 * cb + 16 * kq + (lane >> 3)) * 8192u + tb * 256u + m2 * 128u + (lane & 7) * 16u;
#define W2LD(s, A0, A1) do { const u32 o_ = (s) < 24 ? W2S + (((s) - 12) * 4 + kq) * 2048u : (s) < 26 ? W2E + (kq * 2 + (s) - 24) * 2048u : w * 6144u + ((s) - 26) * 2048u; \
      A0 = cat8(trread(ta + o_), trread(ta + o_ + 1024u)); A1 = cat8(trread(tc + o_), trread(tc + o_ + 1024u)); } while (0)
#define G2STEP(s, N) do { waitv1<N>(hf[s]); \
      if ((s) >= 11 && (s) < 27) W2LD((s) + 1, n0, n1); \
      SB0(); \
      if ((s) < 12) { a0 = wr[2 * ((s) % 12)]; a1 = wr[2 * ((s) % 12) + 1]; } \
      c0 = __builtin_amdgcn_mfma_f32_32x32x16_bf16(a0, __builtin_bit_cast(b8, hf[s]), c0, 0, 0, 0); \
      c1 = __builtin_amdgcn_mfma_f32_32x32x16_bf16(a1, __builtin_bit_cast(b8, hf[s]), c1, 0, 0, 0); \
      a0 = n0; a1 = n1; SB0(); } while (0)
    G2STEP(0, 27); G2STEP(1, 26); G2STEP(2, 25); G2STEP(3, 24);
#pragma unroll
    for (int s = 20; s < 28; ++s) hf[s] = HLOAD(ho, rsW, SIG(s) * 1024u);
    qv[0] = aload8(mrow * 896u + d0 * 2u, rsQ, 0);
    qv[1] = aload8(mrow * 896u + d0 * 2u, rsQ, 16);
    gr = aload8s(OFF_G + mrow * 8u, rsW, 0);
    G2STEP(4, 34); G2STEP(5, 33); G2STEP(6, 32); G2STEP(7, 31); G2STEP(8, 30); G2STEP(9, 29); G2STEP(10, 28); G2STEP(11, 27);
    G2STEP(12, 18 + 8 * (12 < NH0)); G2STEP(13, 17 + 8 * (13 < NH0)); G2STEP(14, 16 + 8 * (14 < NH0)); G2STEP(15, 15 + 8 * (15 < NH0));
    G2STEP(16, 14 + 8 * (16 < NH0)); G2STEP(17, 13 + 8 * (17 < NH0)); G2STEP(18, 12 + 8 * (18 < NH0)); G2STEP(19, 11 + 8 * (19 < NH0));
    G2STEP(20, 10); G2STEP(21, 9);
    yv[0] = aload16s(yo, rsW, 0); yv[1] = aload16s(yo, rsW, 65536u);
    G2STEP(22, 10); G2STEP(23, 9); G2STEP(24, 8); G2STEP(25, 7);
    STAMP(8);
    G2STEP(26, 6); G2STEP(27, 5);
    asm volatile("s_waitcnt vmcnt(2)" : "+v"(qv[0]), "+v"(qv[1]), "+v"(gr)::"memory");
    u64 g64 = mk64(gr);
    if (!__all((g64 >> 16) == (tag & 0xffffffffffffull))) {
      for (int it = 0; it < SPIN_LIM; ++it) {
        g64 = flag_get(ws, OFF_G + mrow * 8u);
        if (__all((g64 >> 16) == (tag & 0xffffffffffffull))) break;
        __builtin_amdgcn_s_sleep(1);
      }
    }
    const float g = bflo((u32)g64 & 0xffffu);
    f4v qd[8];
#pragma unroll
    for (int u = 0; u < 4; ++u) {
      qd[u] = (f4v){c0[4 * u], c0[4 * u + 1], c0[4 * u + 2], c0[4 * u + 3]};
      qd[4 + u] = (f4v){c1[4 * u], c1[4 * u + 1], c1[4 * u + 2], c1[4 * u + 3]};
    }
    const u32 rb = sm + m2 * 24576u;
    red_w(kq, qd, rb, lane);
    bar();
    const u32 yb = sm + W2S + w * 2048u;
    asm volatile("s_waitcnt vmcnt(0)" : "+v"(yv[0]), "+v"(yv[1])::"memory");
    for (int it = 0; it < SPIN_LIM; ++it) {
      if (__all(yv[0][1] == tag32 && yv[0][3] == tag32 && yv[1][1] == tag32 && yv[1][3] == tag32)) break;
      __builtin_amdgcn_s_sleep(1);
      yv[0] = aload16s(yo, rsW, 0); yv[1] = aload16s(yo, rsW, 65536u);
      asm volatile("s_waitcnt vmcnt(0)" : "+v"(yv[0]), "+v"(yv[1])::"memory");
    }
#pragma unroll
    for (int i = 0; i < 2; ++i) *LDSP(u2, yb + (8 * i + (lane >> 3)) * 72u + (lane & 7) * 8u) = (u2){yv[i][0], yv[i][2]};
    f4v r[2];
    red_r(kq, qd, rb, lane, r[0], r[1]);
#pragma unroll
    for (int t = 0; t < 2; ++t) {
      u2 y2 = __builtin_bit_cast(u2, trread(yb + (8 * t + 4 * h + q4) * 72u + (16 * blk + 4 * p4) * 2u));
      float o0 = g * (bflo(qv[t][0]) * bflo(y2[0])) + (1.f - g) * r[t][0];
      float o1 = g * (bfhi(qv[t][0]) * bfhi(y2[0])) + (1.f - g) * r[t][1];
      float o2 = g * (bflo(qv[t][1]) * bflo(y2[1])) + (1.f - g) * r[t][2];
      float o3 = g * (bfhi(qv[t][1]) * bfhi(y2[1])) + (1.f - g) * r[t][3];
      u2 o = {pack2(o0, o1), pack2(o2, o3)};
      __builtin_amdgcn_raw_buffer_store_b64(o, rsO, mrow * 896u + (d0 + 8 * t) * 2u, 0, 0);
    }
  }
  STAMP(9);
}

extern "C" void kernel_launch(void* const* d_in, const int* in_sizes, int n_in, void* d_out, int out_size, void* d_ws, size_t ws_size,
                              hipStream_t stream) {
  fused_kernel<<<256, 512, 0, stream>>>((const char*)d_in[0], (const char*)d_in[1], (const char*)d_in[2], (const char*)d_in[3],
                                        (const char*)d_in[4], (const char*)d_in[5], (const char*)d_in[7], (const char*)d_in[8],
                                        (const char*)d_in[9], (const char*)d_in[10], (char*)d_out, (char*)d_ws);
}
```

```cpp
#include <hip/hip_runtime.h>

typedef unsigned int u32;
typedef unsigned long long u64;
typedef __attribute__((ext_vector_type(2))) __bf16 b2;
typedef __attribute__((ext_vector_type(4))) __bf16 b4;
typedef __attribute__((ext_vector_type(8))) __bf16 b8;
typedef __attribute__((ext_vector_type(2))) unsigned u2;
typedef __attribute__((ext_vector_type(4))) unsigned u4;
typedef __attribute__((ext_vector_type(2))) float f2v;
typedef __attribute__((ext_vector_type(4))) float f4v;
typedef __attribute__((ext_vector_type(16))) float f16v;
typedef __amdgpu_buffer_rsrc_t rsrc_t;
#define LDSP(T, a) ((__attribute__((address_space(3))) T*)(size_t)(a))
#define DI __device__ __forceinline__

#define OFF_G 16384u
#define OFF_HF (OFF_H + 2048u * 1792u * 2u)
#define OFF_KVR 65536u
#define OFF_Y2 (OFF_KVR + 448u * 8192u + 4096u)
#define OFF_H (OFF_Y2 + 448u * 8192u + 4096u)
#define WS_USED (OFF_HF + 1792u * 128u)
#define SPIN_LIM 400000
#define HACQ 0
#define MAP 2
#define DIAG 0
#define ROT 1
#define NH0 12
#define LA 4
#define HLOAD aload16s

#define STAMP(i) do { } while (0)
#define STAMPDECL

DI float bflo(u32 v) { return __uint_as_float(v << 16); }
DI float bfhi(u32 v) { return __uint_as_float(v & 0xffff0000u); }
DI u32 pack2(float lo, float hi) {
  f2v f = {lo, hi};
  b2 r = __builtin_convertvector(f, b2);
  return __builtin_bit_cast(u32, r);
}
DI rsrc_t mkrs(const void* p, u32 bytes) { return __builtin_amdgcn_make_buffer_rsrc((void*)p, 0, bytes, 0x00020000); }
DI void glds16(u32 ldsaddr, u32 voff, rsrc_t rs, u32 soff) {
  asm volatile("s_mov_b32 m0, %0\n\ts_nop 0\n\tbuffer_load_dwordx4 %1, %2, %3 offen lds" ::"s"(ldsaddr), "v"(voff), "s"(rs), "s"(soff) : "memory");
}
DI u4 aload16(u32 voff, rsrc_t rs, u32 soff) {
  u4 r;
  asm volatile("buffer_load_dwordx4 %0, %1, %2, %3 offen" : "=v"(r) : "v"(voff), "s"(rs), "s"(soff) : "memory");
  return r;
}
DI u2 aload8(u32 voff, rsrc_t rs, u32 soff) {
  u2 r;
  asm volatile("buffer_load_dwordx2 %0, %1, %2, %3 offen" : "=v"(r) : "v"(voff), "s"(rs), "s"(soff) : "memory");
  return r;
}
DI void bar() { asm volatile("s_waitcnt lgkmcnt(0)\n\ts_barrier" ::: "memory"); }
DI void vm0() { asm volatile("s_waitcnt vmcnt(0)" ::: "memory"); }
DI void flag_set(char* ws, u32 off, u64 tag) {
  __hip_atomic_store((u64*)(ws + off), tag, __ATOMIC_RELAXED, __HIP_MEMORY_SCOPE_AGENT);
}
DI u64 flag_get(char* ws, u32 off) { return __hip_atomic_load((u64*)(ws + off), __ATOMIC_RELAXED, __HIP_MEMORY_SCOPE_AGENT); }
DI void poll(char* ws, u32 off, u64 tag) {
  for (int it = 0; it < SPIN_LIM; ++it) {
    u64 v = flag_get(ws, off);
    if (__all(v == tag)) break;
    __builtin_amdgcn_s_sleep(1);
  }
  asm volatile("" ::: "memory");
}
DI b8 cat8(b4 a, b4 c) { return __builtin_shufflevector(a, c, 0, 1, 2, 3, 4, 5, 6, 7); }
DI b4 trread(u32 a) { return __builtin_amdgcn_ds_read_tr16_b64_v4bf16(LDSP(b4, a)); }

DI u32 mish2(float h0, float h1) {
  const f2v h = {h0, h1};
  const f2v t = h * 1.44269504088896f;
  const f2v e = {__builtin_amdgcn_exp2f(t[0]), __builtin_amdgcn_exp2f(t[1])};
  const f2v c = {0.70710678118654752f, 0.70710678118654752f}, hf = {0.5f, 0.5f};
  const f2v u = __builtin_elementwise_fma(e, c, c);
  const f2v d = __builtin_elementwise_fma(u, u, hf);
  const f2v r = {__builtin_amdgcn_rcpf(d[0]), __builtin_amdgcn_rcpf(d[1])};
  const f2v o = __builtin_elementwise_fma(-h, r, h);
  return pack2(o[0], o[1]);
}
DI u32 aload4(u32 voff, rsrc_t rs, u32 soff) {
  u32 r;
  asm volatile("buffer_load_dword %0, %1, %2, %3 offen" : "=v"(r) : "v"(voff), "s"(rs), "s"(soff) : "memory");
  return r;
}

template <int Q>
DI void red_write(const f4v (&a)[8], u32 rb, int lane) {
#pragma unroll
  for (int o = 0; o < 4; ++o) {
    if (o == Q) continue;
    const int op = (o - Q - 1) & 3;
    *LDSP(f4v, rb + (Q * 6 + op * 2) * 1024 + lane * 16) = a[2 * o];
    *LDSP(f4v, rb + (Q * 6 + op * 2 + 1) * 1024 + lane * 16) = a[2 * o + 1];
  }
}
template <int Q>
DI void red_read(const f4v (&a)[8], u32 rb, int lane, f4v& r0, f4v& r1) {
  r0 = a[2 * Q];
  r1 = a[2 * Q + 1];
#pragma unroll
  for (int s = 0; s < 4; ++s) {
    if (s == Q) continue;
    const int op = (Q - s - 1) & 3;
    r0 += *LDSP(f4v, rb + (s * 6 + op * 2) * 1024 + lane * 16);
    r1 += *LDSP(f4v, rb + (s * 6 + op * 2 + 1) * 1024 + lane * 16);
  }
}
DI void red_w(int q, const f4v (&a)[8], u32 rb, int lane) {
  if (q == 0) red_write<0>(a, rb, lane);
  else if (q == 1) red_write<1>(a, rb, lane);
  else if (q == 2) red_write<2>(a, rb, lane);
  else red_write<3>(a, rb, lane);
}
DI void red_r(int q, const f4v (&a)[8], u32 rb, int lane, f4v& r0, f4v& r1) {
  if (q == 0) red_read<0>(a, rb, lane, r0, r1);
  else if (q == 1) red_read<1>(a, rb, lane, r0, r1);
  else if (q == 2) red_read<2>(a, rb, lane, r0, r1);
  else red_read<3>(a, rb, lane, r0, r1);
}

#define CV_K 0u
#define CV_R 16896u
#define KST 4224u
#define RST 4576u
#define W2S 65536u
#define W2E 49152u
#define SB0() __builtin_amdgcn_sched_barrier(0)
#define WAITV(n) asm volatile("s_waitcnt vmcnt(" #n ")" ::: "memory")
template <int N>
DI void waitv() { asm volatile("s_waitcnt vmcnt(%0)" ::"n"(N) : "memory"); }
template <int N>
DI void waitv1(u4& a) { asm volatile("s_waitcnt vmcnt(%1)" : "+v"(a) : "n"(N) : "memory"); }
template <int OFF>
DI u4 lds128(u32 a) { u4 r; asm volatile("ds_read_b128 %0, %1 offset:%2" : "=v"(r) : "v"(a), "n"(OFF) : "memory"); return r; }
template <int OFF>
DI u2 lds64(u32 a) { u2 r; asm volatile("ds_read_b64 %0, %1 offset:%2" : "=v"(r) : "v"(a), "n"(OFF) : "memory"); return r; }
DI u4 aload16s(u32 voff, rsrc_t rs, u32 soff) {
  u4 r;
  asm volatile("buffer_load_dwordx4 %0, %1, %2, %3 offen sc1" : "=v"(r) : "v"(voff), "s"(rs), "s"(soff) : "memory");
  return r;
}
DI u2 aload8s(u32 voff, rsrc_t rs, u32 soff) {
  u2 r;
  asm volatile("buffer_load_dwordx2 %0, %1, %2, %3 offen sc1" : "=v"(r) : "v"(voff), "s"(rs), "s"(soff) : "memory");
  return r;
}
DI u64 mk64(u2 v) { return ((u64)v[1] << 32) | v[0]; }
DI b8 mkb8(u2 lo, u2 hi) { u4 t = {lo[0], lo[1], hi[0], hi[1]}; return __builtin_bit_cast(b8, t); }
struct CF { u4 A[8]; u2 L[8], H[8]; };
template <int BB, int I>
DI void cv_load(CF& f, u32 aa, u32 ba) {
  if constexpr (I == 0) f.A[0] = lds128<3584 + BB * 64>(aa);
  else if constexpr (I <= 8) { f.L[I - 1] = lds64<BB * 64 + (I - 1) * 512>(ba); f.H[I - 1] = lds64<BB * 64 + (I - 1) * 512 + 8>(ba); }
  else f.A[I - 8] = lds128<3584 + BB * 64 - (I - 8) * 512>(aa);
}
#define MF16(e, a) cacc[(a) + (e)] = __builtin_amdgcn_mfma_f32_16x16x32_bf16(__builtin_bit_cast(b8, f.A[e]), mkb8(f.L[a], f.H[a]), cacc[(a) + (e)], 0, 0, 0)
#define WAITL(n, ...) asm volatile("s_waitcnt lgkmcnt(" #n ")" : __VA_ARGS__::"memory")

#define PF 13
#define LP 1
#define FS 32u
#define YS 15
#define YB 1
#define SP 0
#define R2M 2
#define PFS 40
__global__ __launch_bounds__(512, 2) void fused_kernel(const char* __restrict__ Q, const char* __restrict__ Kin, const char* __restrict__ V,
                                                       const char* __restrict__ SK, const char* __restrict__ SSMK,
                                                       const char* __restrict__ SSMD, const char* __restrict__ W1,
                                                       const char* __restrict__ B1, const char* __restrict__ W2,
                                                       const char* __restrict__ GW, char* __restrict__ out, char* __restrict__ ws) {
  __shared__ __attribute__((aligned(16))) char smem[163840];
  asm volatile(".Lfk_entry:");
  const u32 sm = (u32)(size_t)smem;
  const int tid = threadIdx.x, lane = tid & 63;
  const int w = __builtin_amdgcn_readfirstlane(tid >> 6);
  const rsrc_t rsQ = mkrs(Q, 2048u * 896u);
  const rsrc_t rsW = mkrs(ws, WS_USED);

  if (blockIdx.x < 32) {
    const u64 tag = flag_get(ws, 0) + 1;
    const int hx = blockIdx.x & 7, hw = (blockIdx.x >> 3) * 8 + w;
    const int row0 = (PF & 8) ? hx * 256 + hw * 8 : blockIdx.x * 64 + w * 8;
    const u32 lo = lane < 56 ? lane * 16u : 0x80000000u;
    u32 pf[9] = {0, 0, 0, 0, 0, 0, 0, 0, 0};
    if (PF & 1) {
      const char* pc;
      u32 po;
      asm volatile("s_getpc_b64 %0\n1:\n\ts_mov_b32 %1, 1b-.Lfk_entry" : "=s"(pc), "=s"(po));
      pf[0] = __builtin_amdgcn_raw_buffer_load_b32(mkrs(pc - po, po), (hw * 64 + lane) * 128u, 0, 0);
    }
    typedef const char* __attribute__((address_space(4))) const* kap_t;
    kap_t ka = (kap_t)__builtin_amdgcn_kernarg_segment_ptr();
    asm volatile("" : "+s"(ka));
    if (PF & 2) pf[1] = __builtin_amdgcn_raw_buffer_load_b32(mkrs(ka[4], 448u * 4096u), hw < 28 ? (hw * 8 + hx) * 8192u + lane * 128u : 0x80000000u, 0, 0);
    if (PF & 16) __builtin_amdgcn_s_sleep(PFS);
    if (PF & 4) {
#pragma unroll
      for (int i = 0; i < 7; ++i)
        pf[2 + i] = __builtin_amdgcn_raw_buffer_load_b32(mkrs(ka[8], 1792u * 896u), (hw * 64 + lane) * 128u, i * 262144u, 0);
    }
    u4 gw = __builtin_amdgcn_raw_buffer_load_b128(mkrs(GW, 896u), lo, 0, 0);
    u4 xr[8];
#pragma unroll
    for (int i = 0; i < 8; ++i) xr[i] = __builtin_amdgcn_raw_buffer_load_b128(rsQ, lo, (row0 + i) * 896u, 0);
    float mine = 0.f;
#pragma unroll
    for (int i = 0; i < 8; ++i) {
      float s = 0.f;
#pragma unroll
      for (int e = 0; e < 4; ++e) s += bflo(xr[i][e]) * bflo(gw[e]) + bfhi(xr[i][e]) * bfhi(gw[e]);
#pragma unroll
      for (int m = 1; m < 64; m <<= 1) s += __shfl_xor(s, m);
      if (lane == i) mine = s;
    }
    if (lane < 8) {
      float g = __builtin_amdgcn_rcpf(1.f + __builtin_amdgcn_exp2f(-1.44269504088896f * mine));
      u64 gr = (tag << 16) | (u64)(pack2(g, 0.f) & 0xffffu);
      flag_set(ws, OFF_G + (row0 + lane) * 8u, gr);
    }
    asm volatile("" ::"v"(pf[0]), "v"(pf[1]), "v"(pf[2]), "v"(pf[3]), "v"(pf[4]), "v"(pf[5]), "v"(pf[6]), "v"(pf[7]), "v"(pf[8]));
    if (blockIdx.x == 0) {
      for (int it = 0; it < (LP ? SPIN_LIM : 0); ++it) {
        if (__all((u32)(flag_get(ws, OFF_Y2) >> 32) == (u32)tag)) break;
        __builtin_amdgcn_s_sleep(16);
      }
      for (int i = 0; i < 4; ++i) {
        int f = tid + i * 512;
        poll(ws, OFF_HF + (f < 1792 ? f : 0) * FS, tag);
      }
      for (int it = 0; it < SPIN_LIM; ++it) {
        u64 v = flag_get(ws, OFF_G + (tid & 255) * 64u);
        if (__all((v >> 16) == (tag & 0xffffffffffffull))) break;
        __builtin_amdgcn_s_sleep(2);
      }
      __syncthreads();
      if (tid == 0) flag_set(ws, 0, tag);
    }
    return;
  }

  const int b = blockIdx.x - 32;
  const int xcd = b & 7, idx = b >> 3;
  const int mt = 4 * (xcd >> 1) + idx / 7, ft = 7 * (xcd & 1) + idx % 7;
  const int rot1 = ROT ? (idx % 7 + 2 * (idx / 7)) % 7 : 0;
  const int tb = 4 * xcd + idx / 7, cb = idx % 7;
  const int rot2 = R2M == 0 ? idx : R2M == 1 ? 4 * cb : R2M == 2 ? 0 : 4 * cb + idx / 7;
#define KAP(it) ((it) + rot1 >= 7 ? (it) + rot1 - 7 : (it) + rot1)
#define SIG(s) ((s) + rot2 >= 28 ? (s) + rot2 - 28 : (s) + rot2)
  const int h = lane >> 5, r31 = lane & 31;
  const int blk = (lane >> 4) & 1, q4 = (lane & 15) >> 2, p4 = lane & 3;
  STAMPDECL;
  STAMP(0);

  const int fg = w & 3, mh = w >> 2;
  const int kq = w & 3, m2 = w >> 2;
  const int t8 = tid & 255;
  u2 bias[4], tg;
  u4 kreg, rr0, rr1;
  const rsrc_t rsW1 = mkrs(W1, 448u * 3584u);
  const rsrc_t rsW2 = mkrs(W2, 1792u * 896u);
  const u32 w2l = (lane >> 3) * 896u + cb * 128u + (((lane & 7) ^ (((lane >> 4) & 1) << 2)) * 16u);
  u32 xo[2], wo[2];
#pragma unroll
  for (int i = 0; i < 2; ++i) {
    int row = 16 * w + 8 * i + (lane >> 3);
    xo[i] = (mt * 128 + row) * 896u + (((lane & 7) ^ ((row >> 1) & 7)) * 16u);
    int rw = 8 * w + 4 * i + (lane >> 4);
    wo[i] = rw * 3584u + ft * 256u + (((lane & 15) ^ (((rw & 3) << 2) | ((rw >> 2) & 3))) * 16u);
  }
#define PART(p) do { const int it_ = (p) >> 2, i_ = (p) & 3; if (it_ < 7) { \
      if (i_ & 1) glds16(sm + (it_ % 5) * 32768u + 16384u + (2 * w + (i_ >> 1)) * 1024u, wo[i_ >> 1], rsW1, KAP(it_) * 64u * 3584u); \
      else glds16(sm + (it_ % 5) * 32768u + (2 * w + (i_ >> 1)) * 1024u, xo[i_ >> 1], rsQ, KAP(it_) * 128u); } \
    else { const int s_ = (it_ - 7) * 4 + i_; \
      glds16(sm + W2S + (s_ * 4 + kq) * 2048u + m2 * 1024u, w2l, rsW2, w2s + SIG(s_) * 16u * 896u); } } while (0)
#define WB(s) glds16(sm + W2S + (((s) - 12) * 4 + kq) * 2048u + m2 * 1024u, w2l, rsW2, w2s + SIG(s) * 16u * 896u)
#define WR(s) do { const u32 o_ = W2S + ((s) * 4 + kq) * 2048u; \
    wr[2 * (s)] = cat8(trread(ta + o_), trread(ta + o_ + 1024u)); wr[2 * (s) + 1] = cat8(trread(tc + o_), trread(tc + o_ + 1024u)); } while (0)
  const u32 w2s = (kq * 448 + m2 * 8) * 896u;
  const u32 ta = sm + (4 * h + q4) * 128u + (((2 * blk + (p4 >> 1)) ^ ((q4 >> 1) << 2)) * 16u) + 8u * (p4 & 1);
  const u32 tc = sm + (4 * h + q4) * 128u + (((2 * blk + (p4 >> 1) + 4) ^ ((q4 >> 1) << 2)) * 16u) + 8u * (p4 & 1);
  b8 wr[24];
#define WAIT2(a, c) asm volatile("s_waitcnt vmcnt(" #a ")\n\ts_cmp_gt_u32 %0, 3\n\ts_cbranch_scc1 1f\n\ts_waitcnt vmcnt(" #c ")\n1:" ::"s"(w) : "scc", "memory")
  PART(0); PART(1); PART(2); PART(3);
  tg = aload8s(0u, rsW, 0u);
  {
    const rsrc_t rsB1 = mkrs(B1, 3584u);
#pragma unroll
    for (int i = 0; i < 4; ++i) bias[i] = aload8((ft * 128 + fg * 32 + 8 * i + 4 * h) * 2u, rsB1, 0);
  }
  const rsrc_t rsSK = mkrs(SSMK, 448u * 4096u);
  const u32 dpair = *(const u32*)(SSMD + b * 4);
  const rsrc_t rsO = mkrs(out, 2048u * 896u);
  u2 kk[7], vv[4];
  u4 sk[2];
  const int pc = 64 * cb + 4 * (lane & 15), pt0 = 64 * tb + 16 * (w & 3) + 4 * (lane >> 4);
  if (w >= 4) {
    const rsrc_t rsK = mkrs(Kin, 2048u * 896u), rsV = mkrs(V, 2048u * 896u);
    const u32 o0 = pt0 * 896u + pc * 2u;
    const u32 om = pt0 > 0 ? o0 - 3 * 896u : 0x80000000u;
    sk[0] = aload16(pc * 8u, mkrs(SK, 3584u), 0);
    sk[1] = aload16(pc * 8u, mkrs(SK, 3584u), 16);
#pragma unroll
    for (int i = 0; i < 3; ++i) kk[i] = aload8(om, rsK, i * 896u);
#pragma unroll
    for (int i = 0; i < 4; ++i) {
      kk[3 + i] = aload8(o0, rsK, i * 896u);
      vv[i] = aload8(o0, rsV, i * 896u);
    }
  }
  PART(4); PART(5); PART(6); PART(7);
  asm volatile("" ::"s"(dpair), "s"(rsO), "s"(rsW2), "s"(rsSK));
  WAIT2(17, 4);
  asm volatile("" : "+v"(bias[0]), "+v"(bias[1]), "+v"(bias[2]), "+v"(bias[3]), "+v"(tg)::"memory");
  const u64 tag = mk64(tg) + 1;
  const u32 tag32 = (u32)tag;
  STAMP(1);

  f16v acc0, acc1;
#pragma unroll
  for (int i = 0; i < 4; ++i) {
    acc0[4 * i] = bflo(bias[i][0]); acc0[4 * i + 1] = bfhi(bias[i][0]);
    acc0[4 * i + 2] = bflo(bias[i][1]); acc0[4 * i + 3] = bfhi(bias[i][1]);
  }
  acc1 = acc0;
  u32 xa[4], wa[2];
#pragma unroll
  for (int s = 0; s < 4; ++s) xa[s] = sm + (64 * mh + r31) * 128u + (((2 * s + h) ^ ((r31 >> 1) & 7)) * 16u);
#pragma unroll
  for (int t = 0; t < 2; ++t)
    wa[t] = sm + 16384u + (8 * h + 4 * t + q4) * 256u + (((4 * fg + 2 * blk + (p4 >> 1)) ^ ((q4 << 2) | ((2 * h + t) & 3))) * 16u) + 8u * (p4 & 1);
  const u32 kvo = OFF_KVR + (2 * b + mh) * 8192u + t8 * 16u;
  b8 af[28], x0[28], x1[28];
#define G1LD(j) do { const u32 so_ = (((j) >> 2) % 5) * 32768u; \
    af[j] = cat8(trread(wa[0] + so_ + ((j) & 3) * 4096u), trread(wa[1] + so_ + ((j) & 3) * 4096u)); \
    x0[j] = *LDSP(b8, xa[(j) & 3] + so_); x1[j] = *LDSP(b8, xa[(j) & 3] + so_ + 4096u); } while (0)
  bar();
  STAMP(10);
  G1LD(0);
  G1LD(1);
#pragma unroll
  for (int j = 0; j < 28; ++j) {
    if ((j & 3) == 2) {
      const int kb = (j + 2) >> 2;
      if (kb == 1) WAITV(6);
      if (kb >= 2 && kb <= 3) WAIT2(14, 10);
      if (kb == 4) WAIT2(15, 11);
      if (kb == 5) WAITV(11);
      if (kb == 6) WAITV(12);
      if (kb == 7) WAITV(4);
      bar();
      if (kb < 7) STAMP(10 + kb);
    }
    if (j + 2 < 28 && DIAG != 1) G1LD(j + 2);
    SB0();
    if (DIAG != 1 || j < 2) {
    acc0 = __builtin_amdgcn_mfma_f32_32x32x16_bf16(af[j], x0[j], acc0, 0, 0, 0);
    acc1 = __builtin_amdgcn_mfma_f32_32x32x16_bf16(af[j], x1[j], acc1, 0, 0, 0);
    }
    SB0();
    if (j == 10) kreg = aload16(t8 * 16u, rsSK, (2 * b + mh) * 4096u);
    if (j == 18) { rr0 = aload16s(kvo, rsW, 0); rr1 = aload16s(kvo, rsW, 4096); }
    if (j < 4) { PART(8 + 3 * j); PART(9 + 3 * j); PART(10 + 3 * j); }
    else if (j < 24) PART(j + 16);
    if (j >= 26) glds16(sm + W2E + (kq * 2 + j - 26) * 2048u + m2 * 1024u, w2l, rsW2, (kq * 448 + m2 * 8 + SIG(j - 2) * 16u) * 896u);
    SB0();
    if (j == 3 && w >= 4) {
      asm volatile("s_waitcnt vmcnt(%13)" : "+v"(sk[0]), "+v"(sk[1]), "+v"(kk[0]), "+v"(kk[1]), "+v"(kk[2]), "+v"(kk[3]), "+v"(kk[4]), "+v"(kk[5]), "+v"(kk[6]),
                   "+v"(vv[0]), "+v"(vv[1]), "+v"(vv[2]), "+v"(vv[3]) : "n"(16) : "memory");
      const u32 so = OFF_KVR + pc * 8192u + (2044 - pt0) * 4u;
#pragma unroll
      for (int c = 0; c < 4; ++c) {
        const u32 s01 = sk[c >> 1][2 * (c & 1)], s23 = sk[c >> 1][2 * (c & 1) + 1];
        float kf[7], a[4];
#pragma unroll
        for (int r = 0; r < 7; ++r) kf[r] = (c & 1) ? bfhi(kk[r][c >> 1]) : bflo(kk[r][c >> 1]);
#pragma unroll
        for (int r = 0; r < 4; ++r) {
          float sv = kf[r] * bfhi(s23) + kf[r + 1] * bflo(s23) + kf[r + 2] * bfhi(s01) + kf[r + 3] * bflo(s01);
          a[r] = sv * ((c & 1) ? bfhi(vv[r][c >> 1]) : bflo(vv[r][c >> 1]));
        }
        __builtin_amdgcn_raw_buffer_store_b128((u4){pack2(a[3], a[2]), tag32, pack2(a[1], a[0]), tag32}, rsW, so + c * 8192u, 0, 16);
      }
      SB0();
    }
  }
  STAMP(2);
  WR(0); WR(1); WR(2); WR(3); WR(4); WR(5); WR(6); WR(7);
  SB0();
  {
    const u32 ho = OFF_H + ((4 * mt + 2 * mh) * 112u + 8 * ft + 2 * fg) * 1024u + lane * 16u;
#pragma unroll
    for (int s = 0; s < 2; ++s) {
      u4 o;
#pragma unroll
      for (int e = 0; e < 4; ++e) o[e] = mish2(acc0[8 * s + 2 * e], acc0[8 * s + 2 * e + 1]);
      __builtin_amdgcn_raw_buffer_store_b128(o, rsW, ho + s * 1024u, 0, 16);
    }
#pragma unroll
    for (int s = 0; s < 2; ++s) {
      u4 o;
#pragma unroll
      for (int e = 0; e < 4; ++e) o[e] = mish2(acc1[8 * s + 2 * e], acc1[8 * s + 2 * e + 1]);
      __builtin_amdgcn_raw_buffer_store_b128(o, rsW, ho + (112u + s) * 1024u, 0, 16);
    }
  }
  const float dcoef = (w >> 2) ? bfhi(dpair) : bflo(dpair);
  STAMP(3);
  {
    asm volatile("s_waitcnt vmcnt(6)" : "+v"(kreg), "+v"(rr0), "+v"(rr1)::"memory");
    for (int it = 0; it < SPIN_LIM; ++it) {
      if (__all(rr0[1] == tag32 && rr0[3] == tag32 && rr1[1] == tag32 && rr1[3] == tag32)) break;
      __builtin_amdgcn_s_sleep(1);
      rr0 = aload16s(kvo, rsW, 0); rr1 = aload16s(kvo, rsW, 4096);
      asm volatile("s_waitcnt vmcnt(0)" : "+v"(rr0), "+v"(rr1)::"memory");
    }
    const u32 kb0 = sm + CV_K + (2 * mh) * KST, kb1 = kb0 + KST;
    *LDSP(u4, kb0 + 64u + t8 * 16u) = kreg;
#pragma unroll
    for (int e = 0; e < 4; ++e) *LDSP(u32, kb1 + 60u + t8 * 16u + e * 4u) = kreg[e];
    const u32 rb0 = sm + CV_R + (2 * mh) * RST, rb1 = rb0 + RST;
#pragma unroll
    for (int i = 0; i < 2; ++i) {
      const u32 e01 = i ? rr1[0] : rr0[0], e23 = i ? rr1[2] : rr0[2];
      const u32 o = t8 * 8u + i * 2048u;
      *LDSP(u2, rb0 + o) = (u2){e01, e23};
      *LDSP(unsigned short, rb1 + o - 2u) = (unsigned short)e01;
      *LDSP(u32, rb1 + o) = __builtin_amdgcn_alignbit(e23, e01, 16);
      *LDSP(unsigned short, rb1 + o + 4u) = (unsigned short)(e23 >> 16);
    }
    if (t8 == 255) *LDSP(unsigned short, rb1 + 4094u) = 0;
    const u4 z4 = {0u, 0u, 0u, 0u};
    if (tid < 112) *LDSP(u4, sm + CV_R + (tid / 28) * RST + 4096u + (tid % 28) * 16u) = z4;
    else if (tid >= 256 && tid < 320) {
      const int u = tid - 256, cpy = u >> 4, e = u & 15;
      if (!((cpy & 1) && e == 15)) *LDSP(u32, sm + CV_K + cpy * KST + e * 4u) = 0u;
    }
  }
  bar();
  STAMP(4);

  u4 hf[28];
  const u32 ho = OFF_H + ((2 * tb + m2) * 112u + 28 * kq) * 1024u + lane * 16u;
  const int d0 = 64 * cb + 16 * kq + 4 * h, mrow = 64 * tb + 32 * m2 + r31;
  u2 qv[2], gr;
  {
    const int ch = w >> 2, bq = w & 3;
    const int i16 = lane & 15, g4 = lane >> 4;
    const u32 aa = sm + CV_R + (2 * ch + 1 - (i16 & 1)) * RST + (2016 - 32 * (i16 >> 1) + 8 * g4) * 2u + bq * 128u - 3584u;
    const u32 ba = sm + CV_K + (2 * ch + 1 - (i16 & 1)) * KST + (2 * i16 + 2 * (i16 & 1) + 8 * g4) * 2u + bq * 128u;
    f4v cacc[8];
#pragma unroll
    for (int t = 0; t < 8; ++t) cacc[t] = (f4v){0.f, 0.f, 0.f, 0.f};
    CF f, n;
    cv_load<0, 0>(f, aa, ba); cv_load<0, 1>(f, aa, ba); cv_load<0, 2>(f, aa, ba); cv_load<0, 3>(f, aa, ba); cv_load<0, 4>(f, aa, ba);
    cv_load<0, 5>(f, aa, ba); cv_load<0, 6>(f, aa, ba); cv_load<0, 7>(f, aa, ba); cv_load<0, 8>(f, aa, ba);
    cv_load<0, 9>(f, aa, ba); cv_load<0, 10>(f, aa, ba); cv_load<0, 11>(f, aa, ba); cv_load<0, 12>(f, aa, ba);
    cv_load<0, 13>(f, aa, ba); cv_load<0, 14>(f, aa, ba); cv_load<0, 15>(f, aa, ba);
    WAITL(15, "+v"(f.A[0]), "+v"(f.L[0]), "+v"(f.H[0]), "+v"(f.L[1]), "+v"(f.H[1]), "+v"(f.L[2]), "+v"(f.H[2]), "+v"(f.L[3]), "+v"(f.H[3]));
    MF16(0, 0); MF16(0, 1); MF16(0, 2); MF16(0, 3);
    SB0();
    WB(12);
    WAITL(6, "+v"(f.A[1]), "+v"(f.L[4]), "+v"(f.H[4]), "+v"(f.L[5]), "+v"(f.H[5]), "+v"(f.L[6]), "+v"(f.H[6]), "+v"(f.L[7]), "+v"(f.H[7]));
    MF16(0, 4); MF16(0, 5); MF16(0, 6); MF16(0, 7); SB0(); WB(13);
    MF16(1, 0); MF16(1, 1); MF16(1, 2); MF16(1, 3); SB0(); WB(14); MF16(1, 4); MF16(1, 5); MF16(1, 6);
    SB0();
    WAITL(0, "+v"(f.A[2]), "+v"(f.A[3]), "+v"(f.A[4]), "+v"(f.A[5]), "+v"(f.A[6]), "+v"(f.A[7]));
    cv_load<1, 0>(n, aa, ba); cv_load<1, 1>(n, aa, ba); cv_load<1, 2>(n, aa, ba); cv_load<1, 3>(n, aa, ba);
    WB(15); WR(8);
    MF16(2, 0); MF16(2, 1); MF16(2, 2); MF16(2, 3); MF16(2, 4); MF16(2, 5);
    SB0();
    cv_load<1, 4>(n, aa, ba); cv_load<1, 5>(n, aa, ba);
    WB(16); WR(9);
    MF16(3, 0); MF16(3, 1); MF16(3, 2); MF16(3, 3); MF16(3, 4);
    SB0();
    cv_load<1, 6>(n, aa, ba); cv_load<1, 7>(n, aa, ba);
    WB(17); WR(10);
    MF16(4, 0); MF16(4, 1); MF16(4, 2); MF16(4, 3);
    SB0();
    cv_load<1, 8>(n, aa, ba); cv_load<1, 9>(n, aa, ba);
    WB(18); WR(11);
    MF16(5, 0); MF16(5, 1); MF16(5, 2);
    SB0();
    cv_load<1, 10>(n, aa, ba); cv_load<1, 11>(n, aa, ba);
    WB(19);
    MF16(6, 0); MF16(6, 1); MF16(7, 0);
    SB0();
    cv_load<1, 12>(n, aa, ba); cv_load<1, 13>(n, aa, ba); cv_load<1, 14>(n, aa, ba); cv_load<1, 15>(n, aa, ba);
    WAITV(8);
    STAMP(17);
    if (lane == 0) flag_set(ws, OFF_HF + ((2 * mt + mh) * 56 + 4 * ft + fg) * FS, tag);
    f = n;
    bar();
    WAITL(6, "+v"(f.A[0]), "+v"(f.L[0]), "+v"(f.H[0]), "+v"(f.L[1]), "+v"(f.H[1]), "+v"(f.L[2]), "+v"(f.H[2]), "+v"(f.L[3]), "+v"(f.H[3]),
          "+v"(f.A[1]), "+v"(f.L[4]), "+v"(f.H[4]), "+v"(f.L[5]), "+v"(f.H[5]), "+v"(f.L[6]), "+v"(f.H[6]), "+v"(f.L[7]), "+v"(f.H[7]));
    MF16(0, 0); MF16(0, 1); MF16(0, 2); SB0(); WB(20); MF16(0, 3); MF16(0, 4); MF16(0, 5); SB0(); WB(21); MF16(0, 6); MF16(0, 7);
    MF16(1, 0); SB0(); WB(22); MF16(1, 1); MF16(1, 2); MF16(1, 3); SB0(); WB(23); MF16(1, 4); MF16(1, 5); MF16(1, 6);
    SB0();
    WAITL(0, "+v"(f.A[2]), "+v"(f.A[3]), "+v"(f.A[4]), "+v"(f.A[5]), "+v"(f.A[6]), "+v"(f.A[7]));
    qv[0] = aload8(mrow * 896u + d0 * 2u, rsQ, 0);
    qv[1] = aload8(mrow * 896u + d0 * 2u, rsQ, 16);
    gr = aload8s(OFF_G + mrow * 8u, rsW, 0);
    u32 kvp[4];
#pragma unroll
    for (int i = 0; i < 4; ++i) {
      const int te = 256 * (2 * bq + (i >> 1)) + 64 * g4 + 32 * (i & 1) + 2 * i16;
      kvp[i] = *LDSP(u32, sm + CV_R + 2 * ch * RST + (2046 - te) * 2u);
    }
    const u32 hfo = OFF_HF + (tb * 56 + kq * 14 + (lane < 14 ? lane : 0)) * FS;
    SB0();
    MF16(2, 0); MF16(2, 1); MF16(2, 2); MF16(2, 3); MF16(2, 4); MF16(2, 5);
    MF16(3, 0); MF16(3, 1); MF16(3, 2); MF16(3, 3); MF16(3, 4);
    MF16(4, 0); MF16(4, 1); MF16(4, 2); MF16(4, 3);
    SB0();
    u2 hfl = aload8s(hfo, rsW, 0);
    bar();
    MF16(5, 0); MF16(5, 1); MF16(5, 2);
    MF16(6, 0); MF16(6, 1); MF16(7, 0);
    SB0();
    STAMP(5);
    const u32 rb = sm + ch * 24576u;
    red_w(bq, cacc, rb, lane);
    asm volatile("s_waitcnt vmcnt(0)" : "+v"(hfl), "+v"(qv[0]), "+v"(qv[1]), "+v"(gr)::"memory");
    if (!__all(mk64(hfl) == tag)) poll(ws, hfo, tag);
    STAMP(7);
#pragma unroll
    for (int s = 0; s < NH0; ++s) hf[s] = HLOAD(ho, rsW, SIG(s) * 1024u);
    bar();
    f4v r0, r1;
    red_r(bq, cacc, rb, lane, r0, r1);
    const u32 yo = OFF_Y2 + (2 * b + ch) * 8192u + bq * 2048u + g4 * 256u + i16 * 8u;
    __builtin_amdgcn_raw_buffer_store_b64((u2){pack2(r0[0] + bfhi(kvp[0]) * dcoef, r0[1] + bflo(kvp[0]) * dcoef), tag32}, rsW, yo, 0, 16);
    __builtin_amdgcn_raw_buffer_store_b64((u2){pack2(r0[2] + bfhi(kvp[1]) * dcoef, r0[3] + bflo(kvp[1]) * dcoef), tag32}, rsW, yo + 128u, 0, 16);
    __builtin_amdgcn_raw_buffer_store_b64((u2){pack2(r1[0] + bfhi(kvp[2]) * dcoef, r1[1] + bflo(kvp[2]) * dcoef), tag32}, rsW, yo + 1024u, 0, 16);
    __builtin_amdgcn_raw_buffer_store_b64((u2){pack2(r1[2] + bfhi(kvp[3]) * dcoef, r1[3] + bflo(kvp[3]) * dcoef), tag32}, rsW, yo + 1152u, 0, 16);
    bar();
  }
  STAMP(6);

  {
    if (SP) __builtin_amdgcn_s_setprio(3);
#pragma unroll
    for (int i = 0; i < 4; ++i) glds16(sm + w * 6144u + i * 1024u, w2l, rsW2, (kq * 448 + SIG(26 + (i >> 1)) * 16 + (i & 1) * 8) * 896u);
#pragma unroll
    for (int s = NH0; s < 20; ++s) hf[s] = HLOAD(ho, rsW, SIG(s) * 1024u);
    u4 yv[2];
    f16v c0, c1;
#pragma unroll
    for (int i = 0; i < 16; ++i) { c0[i] = 0.f; c1[i] = 0.f; }
    b8 a0, a1, n0, n1;
    const u32 yo = OFF_Y2 + (64 * cb + 16 * kq + (lane >> 3)) * 8192u + tb * 256u + m2 * 128u + (lane & 7) * 16u;
#define W2LD(s, A0, A1) do { const u32 o_ = (s) < 24 ? W2S + (((s) - 12) * 4 + kq) * 2048u : (s) < 26 ? W2E + (kq * 2 + (s) - 24) * 2048u : w * 6144u + ((s) - 26) * 2048u; \
      A0 = cat8(trread(ta + o_), trread(ta + o_ + 1024u)); A1 = cat8(trread(tc + o_), trread(tc + o_ + 1024u)); } while (0)
#define G2STEP(s, N) do { waitv1<N>(hf[s]); \
      if ((s) >= 11 && (s) < 27) W2LD((s) + 1, n0, n1); \
      SB0(); \
      if ((s) < 12) { a0 = wr[2 * ((s) % 12)]; a1 = wr[2 * ((s) % 12) + 1]; } \
      c0 = __builtin_amdgcn_mfma_f32_32x32x16_bf16(a0, __builtin_bit_cast(b8, hf[s]), c0, 0, 0, 0); \
      c1 = __builtin_amdgcn_mfma_f32_32x32x16_bf16(a1, __builtin_bit_cast(b8, hf[s]), c1, 0, 0, 0); \
      a0 = n0; a1 = n1; SB0(); } while (0)
    G2STEP(0, 19 + 8 * (0 < NH0)); G2STEP(1, 18 + 8 * (1 < NH0)); G2STEP(2, 17 + 8 * (2 < NH0)); G2STEP(3, 16 + 8 * (3 < NH0));
#pragma unroll
    for (int s = 20; s < 28; ++s) hf[s] = HLOAD(ho, rsW, SIG(s) * 1024u);
    if (SP == 1) __builtin_amdgcn_s_setprio(0);
    if (SP == 2 && w < 4) __builtin_amdgcn_s_setprio(0);
#define G2Y(s, N) do { G2STEP(s, (N) - 3 + 2 * ((s) > YS)); \
      if ((s) == YS) { yv[0] = aload16s(yo, rsW, 0); yv[1] = aload16s(yo, rsW, 65536u); } } while (0)
    G2Y(4, 26 + 8 * (4 < NH0)); G2Y(5, 25 + 8 * (5 < NH0)); G2Y(6, 24 + 8 * (6 < NH0)); G2Y(7, 23 + 8 * (7 < NH0));
    G2Y(8, 22 + 8 * (8 < NH0)); G2Y(9, 21 + 8 * (9 < NH0)); G2Y(10, 20 + 8 * (10 < NH0)); G2Y(11, 19 + 8 * (11 < NH0));
    G2Y(12, 18 + 8 * (12 < NH0)); G2Y(13, 17 + 8 * (13 < NH0)); G2Y(14, 16 + 8 * (14 < NH0)); G2Y(15, 15 + 8 * (15 < NH0));
    G2Y(16, 14 + 8 * (16 < NH0)); G2Y(17, 13 + 8 * (17 < NH0)); G2Y(18, 12 + 8 * (18 < NH0)); G2Y(19, 11 + 8 * (19 < NH0));
    G2Y(20, 10); G2Y(21, 9);
    G2STEP(22, 7); G2STEP(23, 6); G2STEP(24, 5); G2STEP(25, 4);
    STAMP(8);
    G2STEP(26, 3); G2STEP(27, 2);
    u64 g64 = mk64(gr);
    if (!__all((g64 >> 16) == (tag & 0xffffffffffffull))) {
      for (int it = 0; it < SPIN_LIM; ++it) {
        g64 = flag_get(ws, OFF_G + mrow * 8u);
        if (__all((g64 >> 16) == (tag & 0xffffffffffffull))) break;
        __builtin_amdgcn_s_sleep(1);
      }
    }
    const float g = bflo((u32)g64 & 0xffffu);
    f4v qd[8];
#pragma unroll
    for (int u = 0; u < 4; ++u) {
      qd[u] = (f4v){c0[4 * u], c0[4 * u + 1], c0[4 * u + 2], c0[4 * u + 3]};
      qd[4 + u] = (f4v){c1[4 * u], c1[4 * u + 1], c1[4 * u + 2], c1[4 * u + 3]};
    }
    const u32 rb = sm + m2 * 24576u;
    red_w(kq, qd, rb, lane);
    if (!YB) bar();
    const u32 yb = sm + W2S + w * 2048u;
    asm volatile("s_waitcnt vmcnt(0)" : "+v"(yv[0]), "+v"(yv[1])::"memory");
    for (int it = 0; it < SPIN_LIM; ++it) {
      if (__all(yv[0][1] == tag32 && yv[0][3] == tag32 && yv[1][1] == tag32 && yv[1][3] == tag32)) break;
      __builtin_amdgcn_s_sleep(1);
      yv[0] = aload16s(yo, rsW, 0); yv[1] = aload16s(yo, rsW, 65536u);
      asm volatile("s_waitcnt vmcnt(0)" : "+v"(yv[0]), "+v"(yv[1])::"memory");
    }
    if (YB) bar();
#pragma unroll
    for (int i = 0; i < 2; ++i) *LDSP(u2, yb + (8 * i + (lane >> 3)) * 72u + (lane & 7) * 8u) = (u2){yv[i][0], yv[i][2]};
    f4v r[2];
    red_r(kq, qd, rb, lane, r[0], r[1]);
#pragma unroll
    for (int t = 0; t < 2; ++t) {
      u2 y2 = __builtin_bit_cast(u2, trread(yb + (8 * t + 4 * h + q4) * 72u + (16 * blk + 4 * p4) * 2u));
      float o0 = g * (bflo(qv[t][0]) * bflo(y2[0])) + (1.f - g) * r[t][0];
      float o1 = g * (bfhi(qv[t][0]) * bfhi(y2[0])) + (1.f - g) * r[t][1];
      float o2 = g * (bflo(qv[t][1]) * bflo(y2[1])) + (1.f - g) * r[t][2];
      float o3 = g * (bfhi(qv[t][1]) * bfhi(y2[1])) + (1.f - g) * r[t][3];
      u2 o = {pack2(o0, o1), pack2(o2, o3)};
      __builtin_amdgcn_raw_buffer_store_b64(o, rsO, mrow * 896u + (d0 + 8 * t) * 2u, 0, 0);
    }
  }
  STAMP(9);
}

extern "C" void kernel_launch(void* const* d_in, const int* in_sizes, int n_in, void* d_out, int out_size, void* d_ws, size_t ws_size,
                              hipStream_t stream) {
  fused_kernel<<<256, 512, 0, stream>>>((const char*)d_in[0], (const char*)d_in[1], (const char*)d_in[2], (const char*)d_in[3],
                                        (const char*)d_in[4], (const char*)d_in[5], (const char*)d_in[7], (const char*)d_in[8],
                                        (const char*)d_in[9], (const char*)d_in[10], (char*)d_out, (char*)d_ws);
}
```
